```python
import torch

def _softmax(x, axis=-1):
    return torch.softmax(x, dim=axis)

def _gelu(x):
    return torch.nn.functional.gelu(x, approximate='tanh')

def _silu(x):
    return torch.nn.functional.silu(x)

def _sigmoid(x):
    return torch.sigmoid(x)

def _layernorm(x, axis=-1, eps=1e-05):
    m = x.mean(dim=axis, keepdim=True)
    v = x.var(dim=axis, keepdim=True, unbiased=False)
    return (x - m) / torch.sqrt(v + eps)

def _scatter_add(tgt, idx, src, axis=0):
    return tgt.clone().index_add_(axis, idx, src)

def _segment_sum(src, seg, n):
    out = torch.zeros((n,) + src.shape[1:], dtype=src.dtype)
    return out.index_add_(0, seg, src)

def _pad(x, pw):
    flat = tuple((v for pair in reversed(pw) for v in pair))
    return torch.nn.functional.pad(x, flat)
kernel_name = 'adaLN_cond__grouped_kv_mqa__dense_activation_relu__prompt'

def setup_inputs():

    _w = lambda a: (lambda t: t.to(torch.bfloat16) if t.is_floating_point() else t)(torch.as_tensor(a))

    def _A_setup():
        B, S, D, H = (1, 2048, 576, 32)
        Dh = D // H
        return {'x': _w((lambda *s: torch.randn(*s, dtype=torch.bfloat16) * 0.05)(B, S, D)), 'wq': _w((lambda *s: torch.randn(*s, dtype=torch.bfloat16) * 0.05)(D, H * Dh)), 'wk': _w((lambda *s: torch.randn(*s, dtype=torch.bfloat16) * 0.05)(D, Dh)), 'wv': _w((lambda *s: torch.randn(*s, dtype=torch.bfloat16) * 0.05)(D, Dh)), 'wo': _w((lambda *s: torch.randn(*s, dtype=torch.bfloat16) * 0.05)(H * Dh, D))}
    _A_in = _A_setup()

    def _B_setup():
        B, S, D, Dff = (1, 2048, 576, 4608)
        return {'x': _w((lambda *s: torch.randn(*s, dtype=torch.bfloat16) * 0.05)(B, S, D)), 'w1': _w((lambda *s: torch.randn(*s, dtype=torch.bfloat16) * 0.05)(D, Dff)), 'w2': _w((lambda *s: torch.randn(*s, dtype=torch.bfloat16) * 0.05)(Dff, D))}
    _B_in = _B_setup()
    return {'A_x': _A_in['x'], 'A_wq': _A_in['wq'], 'A_wk': _A_in['wk'], 'A_wv': _A_in['wv'], 'A_wo': _A_in['wo'], 'B_x': _B_in['x'], 'B_w1': _B_in['w1'], 'B_w2': _B_in['w2'], 'c': _w((lambda *s: torch.randn(*s, dtype=torch.bfloat16) * 0.1)(1, 576) * 8.0), 'w_mod': _w((lambda *s: torch.randn(*s, dtype=torch.bfloat16) * 0.1)(576, 6 * 576) * 576 ** (-0.5) * 8.0)}

def reference(A_x, A_wq, A_wk, A_wv, A_wo, B_x, B_w1, B_w2, c, w_mod):

    def _A_ref(x, wq, wk, wv, wo):
        B, S, D = x.shape
        H = 32
        Dh = D // H
        q = (lambda x, *d: x.permute(*d))((x @ wq).reshape(B, S, H, Dh), 0, 2, 1, 3)
        k = x @ wk
        v = x @ wv
        scores = torch.einsum('bhqd,bkd->bhqk', q, k) * Dh ** (-0.5)
        p = _softmax(scores, axis=-1)
        o = torch.einsum('bhqk,bkd->bhqd', p, v)
        return (lambda x, *d: x.permute(*d))(o, 0, 2, 1, 3).reshape(B, S, H * Dh) @ wo

    def _B_ref(x, w1, w2):
        h = torch.einsum('bsd,df->bsf', x, w1)
        zero = torch.zeros_like(h)
        h = torch.maximum(h, zero)
        return torch.einsum('bsf,fd->bsd', h, w2)
    x = A_x
    mod = (_silu(c) @ w_mod).reshape(c.shape[0], 6, 1, -1)
    sa, ca, ga, sb, cb, gb = (mod[:, 0].clone(), mod[:, 1].clone(), mod[:, 2].clone(), mod[:, 3].clone(), mod[:, 4].clone(), mod[:, 5].clone())
    h = _layernorm(x, axis=-1) * (1 + ca) + sa
    x = x + ga * _A_ref(h, A_wq, A_wk, A_wv, A_wo)
    h = _layernorm(x, axis=-1) * (1 + cb) + sb
    x = x + gb * _B_ref(h, B_w1, B_w2)
    return x
```

```cpp
#include <hip/hip_runtime.h>
#include <stdint.h>

typedef unsigned short u16;
typedef __attribute__((ext_vector_type(8))) __bf16 bf16x8;
typedef __attribute__((ext_vector_type(16))) __bf16 bf16x16;
typedef __attribute__((ext_vector_type(2))) __bf16 bf16x2;
typedef __attribute__((ext_vector_type(2))) float f32x2;
typedef __attribute__((ext_vector_type(4))) float f32x4;
typedef __attribute__((ext_vector_type(8))) float f32x8;
typedef __attribute__((ext_vector_type(16))) float f32x16;
typedef __attribute__((ext_vector_type(4))) unsigned u32x4;
typedef __attribute__((ext_vector_type(2))) unsigned u32x2;
#define LAS __attribute__((address_space(3)))
#define SBAR() __builtin_amdgcn_sched_barrier(0)
#define PIN(x) asm volatile("" : "+v"(x))

constexpr int SEQ = 2048, DM = 576, NH = 32, FF = 4608;
constexpr size_t KB = 1024;
constexpr size_t MOD_OFF = 0;
constexpr size_t WQKV_OFF = 16 * KB;
constexpr size_t WO_OFF = WQKV_OFF + 720 * KB;
constexpr size_t W12_OFF = WO_OFF + 37 * 18 * KB;
constexpr size_t QM_OFF = W12_OFF + 32 * 324 * KB;
constexpr size_t QX_OFF = QM_OFF + 2048 * KB;
constexpr size_t KV_OFF = QX_OFF + 256 * KB;
constexpr size_t O_OFF = KV_OFF + 160 * KB;
constexpr size_t X1_OFF = O_OFF + 128 * 18 * KB;
constexpr size_t PART_OFF = X1_OFF + 128 * 18 * KB;
constexpr int KVG = 9472;

__device__ __forceinline__ float bf2f(u16 v) { return __uint_as_float((unsigned)v << 16); }
__device__ __forceinline__ float bflo(unsigned v) { return __uint_as_float(v << 16); }
__device__ __forceinline__ float bfhi(unsigned v) { return __uint_as_float(v & 0xffff0000u); }
__device__ __forceinline__ unsigned pack2(float lo, float hi) {
  f32x2 v = {lo, hi};
  return __builtin_bit_cast(unsigned, __builtin_convertvector(v, bf16x2));
}
__device__ __forceinline__ float rbf(float v) { return (float)(__bf16)v; }
__device__ __forceinline__ u16 f2bf(float v) { return __builtin_bit_cast(u16, (__bf16)v); }
__device__ __forceinline__ f32x4 mfma16(u32x4 a, u32x4 b, f32x4 c) {
  return __builtin_amdgcn_mfma_f32_16x16x32_bf16(__builtin_bit_cast(bf16x8, a), __builtin_bit_cast(bf16x8, b), c, 0, 0, 0);
}
__device__ __forceinline__ f32x16 mfma32(u32x4 a, u32x4 b, f32x16 c) {
  return __builtin_amdgcn_mfma_f32_32x32x16_bf16(__builtin_bit_cast(bf16x8, a), __builtin_bit_cast(bf16x8, b), c, 0, 0, 0);
}
__device__ __forceinline__ f32x16 smf_lo(u32x4 a, f32x16 p, f32x16 c, int idx) {
  f32x8 h = __builtin_shufflevector(p, p, 0, 1, 2, 3, 4, 5, 6, 7);
  return __builtin_amdgcn_smfmac_f32_32x32x32_bf16(__builtin_bit_cast(bf16x8, a), __builtin_bit_cast(bf16x16, h), c, idx, 0, 0);
}
__device__ __forceinline__ f32x16 smf_hi(u32x4 a, f32x16 p, f32x16 c, int idx) {
  f32x8 h = __builtin_shufflevector(p, p, 8, 9, 10, 11, 12, 13, 14, 15);
  return __builtin_amdgcn_smfmac_f32_32x32x32_bf16(__builtin_bit_cast(bf16x8, a), __builtin_bit_cast(bf16x16, h), c, idx, 0, 0);
}
__device__ __forceinline__ void glds16(const void* g, unsigned lds_base) {
  unsigned sv;
  asm volatile("s_mov_b32 %0, m0\n\ts_mov_b32 m0, %2\n\ts_nop 0\n\tglobal_load_lds_dwordx4 %1, off\n\ts_mov_b32 m0, %0"
               : "=&s"(sv) : "v"(g), "s"(lds_base) : "memory");
}
__device__ __forceinline__ int vpos(int d) {
  return d < 4 ? d : d < 12 ? d + 6 : d < 16 ? d - 8 : d == 16 ? 18 : 8;
}

constexpr int K1_MOD = 224, K1_QKV = 180, K1_WO = 162;
__global__ __launch_bounds__(256) void k_prep(const u16* __restrict__ c, const u16* __restrict__ wmod, const u16* __restrict__ wq,
                                              const u16* __restrict__ wk, const u16* __restrict__ wv, const u16* __restrict__ wo,
                                              char* __restrict__ ws) {
  __shared__ float red[128 * 17 + 64];
  const int b = blockIdx.x, t = threadIdx.x;
  if (b < K1_MOD) {
    const int slot = b >> 3, lg = (slot >> 2) * 8 + (b & 7);
    if (lg >= 54) {
      if (b == 223) {
        const int step = t >> 1, hA = t & 1;
        u32x4 ones = {0x3F803F80u, 0x3F803F80u, 0x3F803F80u, 0x3F803F80u};
        *(u32x4*)(ws + KV_OFF + (step >> 3) * KVG + 4608 + (step & 7) * 608 + hA * 304 + 9 * 16) = ones;
      }
      return;
    }
    const int cg = lg * 4 + (slot & 3), half = t & 1, rg = t >> 1, col0 = cg * 16 + half * 8;
    u32x4 w[5]; u16 cv[5];
#pragma unroll
    for (int i = 0; i < 5; ++i) {
      int row = rg + 128 * i; row = row < DM ? row : DM - 1;
      w[i] = *(const u32x4*)(wmod + (size_t)row * 3456 + col0);
      cv[i] = c[row];
    }
    float acc[8] = {0, 0, 0, 0, 0, 0, 0, 0};
#pragma unroll
    for (int i = 0; i < 5; ++i) {
      const float x = bf2f(cv[i]);
      float s = rbf(x / (1.0f + __expf(-x)));
      s = (rg + 128 * i < DM) ? s : 0.0f;
#pragma unroll
      for (int e = 0; e < 4; ++e) { acc[2 * e] += s * bflo(w[i][e]); acc[2 * e + 1] += s * bfhi(w[i][e]); }
    }
#pragma unroll
    for (int e = 0; e < 8; ++e) red[rg * 17 + half * 8 + e] = acc[e];
    __syncthreads();
    const int col = t & 15, part = t >> 4;
    float s = 0;
#pragma unroll
    for (int i = 0; i < 8; ++i) s += red[(part * 8 + i) * 17 + col];
    s += __shfl_xor(s, 16); s += __shfl_xor(s, 32);
    if ((t & 63) < 16) red[128 * 17 + (t >> 6) * 16 + col] = s;
    __syncthreads();
    if (t < 16) {
      const float v = (red[128 * 17 + t] + red[128 * 17 + 16 + t]) + (red[128 * 17 + 32 + t] + red[128 * 17 + 48 + t]);
      ((float*)(ws + MOD_OFF))[cg * 16 + t] = rbf(v);
    }
  } else if (b < K1_MOD + K1_QKV) {
    const int id = (b - K1_MOD) * 256 + t, lane = id & 63, f = id >> 6;
    const int ks = f % 18, jt = f / 18, tile = jt % 5, j = jt / 5, cc = lane & 15, k0 = 32 * ks + 8 * (lane >> 4);
    const u16* src = wq; int col = 0, ld = DM; bool valid = true;
    if (tile < 4) col = (4 * j + tile) * 18 + cc;
    else if (cc < 8) col = (4 * j + (cc >> 1)) * 18 + 16 + (cc & 1);
    else {
      const int jj = j < 3 ? j : j - 3;
      src = j < 3 ? wk : wv; ld = 18; col = jj * 8 + (cc - 8);
      valid = j < 6 && col < 18;
      col = valid ? col : 0;
    }
    u16 v[8];
#pragma unroll
    for (int e = 0; e < 8; ++e) v[e] = src[(size_t)(k0 + e) * ld + col];
    u32x4 o;
#pragma unroll
    for (int e = 0; e < 4; ++e) o[e] = valid ? ((unsigned)v[2 * e] | ((unsigned)v[2 * e + 1] << 16)) : 0u;
    *(u32x4*)(ws + WQKV_OFF + (size_t)id * 16) = o;
  } else {
    const int id = (b - K1_MOD - K1_QKV) * 256 + t, lane = id & 63, f = id >> 6;
    const int ks = f % 18, nt = f / 18, n = 16 * nt + (lane & 15), k0 = 32 * ks + 8 * (lane >> 4);
    u16 v[8];
#pragma unroll
    for (int e = 0; e < 8; ++e) {
      const int k = k0 + e;
      const int row = k < 512 ? (k >> 4) * 18 + (e & 3) + 8 * (e >> 2) + 4 * ((k >> 3) & 1) : ((k - 512) >> 1) * 18 + 16 + (k & 1);
      v[e] = wo[(size_t)row * DM + n];
    }
    u32x4 o;
#pragma unroll
    for (int e = 0; e < 4; ++e) o[e] = (unsigned)v[2 * e] | ((unsigned)v[2 * e + 1] << 16);
    *(u32x4*)(ws + WO_OFF + (size_t)id * 16) = o;
  }
}

__device__ __forceinline__ void ln_mod(u32x4 (&xf)[18], const LAS float* cs, int lane) {
  float sum = 0;
#pragma unroll
  for (int s = 0; s < 18; ++s)
#pragma unroll
    for (int e = 0; e < 4; ++e) sum += bflo(xf[s][e]) + bfhi(xf[s][e]);
  sum += __shfl_xor(sum, 16); sum += __shfl_xor(sum, 32);
  const float mu = sum * (1.0f / DM);
  float sq = 0;
#pragma unroll
  for (int s = 0; s < 18; ++s)
#pragma unroll
    for (int e = 0; e < 4; ++e) { const float a = bflo(xf[s][e]) - mu, bb = bfhi(xf[s][e]) - mu; sq += a * a; sq += bb * bb; }
  sq += __shfl_xor(sq, 16); sq += __shfl_xor(sq, 32);
  const float r = rsqrtf(sq * (1.0f / DM) + 1e-5f), nmr = -mu * r;
  const LAS float* cp = cs + 8 * (lane >> 4);
#pragma unroll
  for (int s = 0; s < 18; ++s) {
    const f32x4 c0 = *(const LAS f32x4*)(cp + 32 * s), c1 = *(const LAS f32x4*)(cp + 32 * s + 4);
    const f32x4 s0 = *(const LAS f32x4*)(cp + DM + 32 * s), s1 = *(const LAS f32x4*)(cp + DM + 32 * s + 4);
    u32x4 o;
    o[0] = pack2((bflo(xf[s][0]) * r + nmr) * c0[0] + s0[0], (bfhi(xf[s][0]) * r + nmr) * c0[1] + s0[1]);
    o[1] = pack2((bflo(xf[s][1]) * r + nmr) * c0[2] + s0[2], (bfhi(xf[s][1]) * r + nmr) * c0[3] + s0[3]);
    o[2] = pack2((bflo(xf[s][2]) * r + nmr) * c1[0] + s1[0], (bfhi(xf[s][2]) * r + nmr) * c1[1] + s1[1]);
    o[3] = pack2((bflo(xf[s][3]) * r + nmr) * c1[2] + s1[2], (bfhi(xf[s][3]) * r + nmr) * c1[3] + s1[3]);
    xf[s] = o;
  }
}

__global__ __launch_bounds__(256) void k_qkv(const u16* __restrict__ x, char* __restrict__ ws) {
  __shared__ __attribute__((aligned(16))) char smem[90 * 1024 + 2 * DM * 4];
  LAS char* L = (LAS char*)smem;
  const int tid = threadIdx.x, lane = tid & 63, w = __builtin_amdgcn_readfirstlane(tid >> 6);
  const int j = blockIdx.x & 7, g = blockIdx.x >> 3;
  const int tok = 64 * g + 16 * w + (lane & 15), g4 = lane >> 4;
  u32x4 xf[18];
#pragma unroll
  for (int s = 0; s < 18; ++s) xf[s] = *(const u32x4*)(x + (size_t)tok * DM + 32 * s + 8 * g4);
  const float* mod = (const float*)(ws + MOD_OFF);
  f32x4 mv = {0, 0, 0, 0}, mv2 = {0, 0, 0, 0};
  if (tid < 144) { mv = *(const f32x4*)(mod + DM + 4 * tid); mv2 = *(const f32x4*)(mod + 4 * tid); }
  SBAR();
  u32x4 wf[23];
#pragma unroll
  for (int i = 0; i < 23; ++i) {
    int f = w + 4 * i; f = f < 90 ? f : 89;
    wf[i] = *(const u32x4*)(ws + WQKV_OFF + (size_t)(j * 90 + f) * 1024 + lane * 16);
  }
  SBAR();
  LAS float* cs = (LAS float*)(L + 90 * 1024);
  if (tid < 144) {
#pragma unroll
    for (int e = 0; e < 4; ++e) mv[e] = rbf(1.0f + mv[e]);
    *(LAS f32x4*)(cs + 4 * tid) = mv;
    *(LAS f32x4*)(cs + DM + 4 * tid) = mv2;
  }
  __syncthreads();
  ln_mod(xf, cs, lane);
#pragma unroll
  for (int i = 0; i < 23; ++i) {
    int f = w + 4 * i; f = f < 90 ? f : 89;
    *(LAS u32x4*)(L + f * 1024 + lane * 16) = wf[i];
  }
  __syncthreads();
  f32x4 acc[5];
#pragma unroll
  for (int t = 0; t < 5; ++t) acc[t] = f32x4{0, 0, 0, 0};
#pragma unroll
  for (int s = 0; s < 18; ++s)
#pragma unroll
    for (int t = 0; t < 5; ++t) acc[t] = mfma16(*(const LAS u32x4*)(L + (t * 18 + s) * 1024 + lane * 16), xf[s], acc[t]);
  const float qs = 0.23570226039551584f * 1.4426950408889634f;
  const int tok32 = (w & 1) * 16 + (lane & 15);
#pragma unroll
  for (int t = 0; t < 4; ++t) {
    const int qt = (g * 32 + 4 * j + t) * 2 + (w >> 1);
    u32x2 o = {pack2(acc[t][0] * qs, acc[t][1] * qs), pack2(acc[t][2] * qs, acc[t][3] * qs)};
    *(u32x2*)(ws + QM_OFF + (size_t)qt * 1024 + ((g4 >> 1) * 32 + tok32) * 16 + (g4 & 1) * 8) = o;
  }
  const f32x4 a = acc[4];
  if (g4 < 2) {
    const int qt = (g * 32 + 4 * j + 2 * g4) * 2 + (w >> 1);
    *(unsigned*)(ws + QX_OFF + (size_t)qt * 128 + tok32 * 4) = pack2(a[0] * qs, a[1] * qs);
    *(unsigned*)(ws + QX_OFF + (size_t)(qt + 2) * 128 + tok32 * 4) = pack2(a[2] * qs, a[3] * qs);
  } else {
    char* kv = ws + KV_OFF + (tok >> 7) * KVG;
    const int tile = (tok >> 5) & 3;
    if (j < 2) {
      u32x2 o = {pack2(a[0], a[1]), pack2(a[2], a[3])};
      *(u32x2*)(kv + tile * 1024 + (j * 32 + (tok & 31)) * 16 + (g4 - 2) * 8) = o;
    } else if (j == 2) {
      if (g4 == 2) *(unsigned*)(kv + 4096 + (tok & 31) * 16 + tile * 4) = pack2(a[0], a[1]);
    } else if (j < 6) {
      char* vb = kv + 4608 + ((tok >> 4) & 7) * 608 + ((tok >> 3) & 1) * 304 + (tok & 7) * 2;
#pragma unroll
      for (int r = 0; r < 4; ++r) {
        const int d = (j - 3) * 8 + (g4 - 2) * 4 + r;
        if (d < 18) *(u16*)(vb + vpos(d) * 16) = f2bf(a[r]);
      }
    }
  }
}

constexpr int ATT_LDS = 16 * KVG + 5120;
__device__ __forceinline__ void retile_load(const u16* __restrict__ w1, const u16* __restrict__ w2, int u, int lane, u32x2 (&v)[8]) {
  const int g4 = lane >> 4, cc = lane & 15;
  const bool second = u >= 1296;
  const int uu = second ? u - 1296 : u, blk = uu % 9, ks = (uu / 9) % 18, slice = uu / 162;
#pragma unroll
  for (int e = 0; e < 8; ++e) {
    const u16* p = second ? w2 + (size_t)(576 * slice + 64 * (ks >> 1) + 16 * g4 + 4 * (e & 3) + 2 * (ks & 1) + (e >> 2)) * DM + 64 * blk + 4 * cc
                          : w1 + (size_t)(32 * ks + 8 * g4 + e) * FF + 576 * slice + 64 * blk + 4 * cc;
    v[e] = *(const u32x2*)p;
  }
}
__device__ __forceinline__ void retile_store(char* __restrict__ ws, int u, int lane, const u32x2 (&v)[8]) {
  const bool second = u >= 1296;
  const int uu = second ? u - 1296 : u, blk = uu % 9, ks = (uu / 9) % 18, slice = uu / 162;
#pragma unroll
  for (int q = 0; q < 4; ++q) {
    const int T = 4 * blk + q, wv = T / 9, tt = T % 9;
    u32x4 o;
#pragma unroll
    for (int e = 0; e < 4; ++e)
      o[e] = __builtin_amdgcn_perm(v[2 * e + 1][q >> 1], v[2 * e][q >> 1], (q & 1) ? 0x07060302u : 0x05040100u);
    *(u32x4*)(ws + W12_OFF + (size_t)((slice * 4 + wv) * 324 + (second ? 162 : 0) + (tt / 3) * 54 + ks * 3 + tt % 3) * 1024 + lane * 16) = o;
  }
}

__global__ __launch_bounds__(512, 2) void k_attn(const u16* __restrict__ w1, const u16* __restrict__ w2, char* __restrict__ ws) {
  __shared__ __attribute__((aligned(16))) char smem[ATT_LDS];
  LAS char* L = (LAS char*)smem;
  const int tid = threadIdx.x, lane = tid & 63, w = __builtin_amdgcn_readfirstlane(tid >> 6);
  const int j = blockIdx.x & 7, g = blockIdx.x >> 3, hi = lane >> 5, r32 = lane & 31;
  const int head = 4 * j + (w >> 1), qt = (g * 32 + head) * 2 + (w & 1);
  u32x4 q1; unsigned qx;
  {
    const char* qp = ws + QM_OFF + (size_t)qt * 1024 + lane * 16;
    const char* qxp = ws + QX_OFF + (size_t)qt * 128 + r32 * 4;
    asm volatile("global_load_dwordx4 %0, %2, off\n\tglobal_load_dword %1, %3, off" : "=&v"(q1), "=&v"(qx) : "v"(qp), "v"(qxp) : "memory");
  }
  const unsigned lds0 = (unsigned)(uintptr_t)L;
#pragma unroll
  for (int i = 0; i < 19; ++i) {
    int p = w + 8 * i; p = p < 148 ? p : p - 8;
    glds16(ws + KV_OFF + (size_t)p * 1024 + lane * 16, lds0 + p * 1024);
  }
  asm volatile("s_waitcnt vmcnt(14)\n\ts_barrier" : "+v"(q1), "+v"(qx)::"memory");
  qx = hi ? 0u : qx;
  u32x4 q2[4];
#pragma unroll
  for (int i = 0; i < 4; ++i) { q2[i] = u32x4{0, 0, 0, 0}; q2[i][i] = qx; PIN(q2[i]); }
  const int rho = r32;
  const int vp = rho < 17 ? vpos(rho) : rho == 20 ? 8 : 9;
  const LAS char* kp = L + lane * 16;
  const LAS char* kxp = L + 4096 + r32 * 16;
  const LAS char* vpp = L + 4608 + hi * 304 + vp * 16;
  int idx = 0xDDDDDDDD; PIN(idx);
#define LDK(G, t) (*(const LAS u32x4*)(kp + (G) + (t) * 1024))
#define LDX(G) (*(const LAS u32x4*)(kxp + (G)))
#define LDV(G, s) (*(const LAS u32x4*)(vpp + (G) + (s) * 608))
  float m0;
  {
    f32x16 z = {};
    f32x16 s = mfma32(LDX(0), q2[0], z);
    s = mfma32(LDK(0, 0), q1, s);
    m0 = s[0];
#pragma unroll
    for (int r = 1; r < 16; ++r) m0 = fmaxf(m0, s[r]);
    m0 = fmaxf(m0, __shfl_xor(m0, 32));
  }
  u32x2 rv0[8], rv1[8];
  const int u0 = blockIdx.x + 256 * w, u1 = u0 + 2048;
  const bool has1 = w < 3 && u1 < 2592;
  f32x16 O;
  for (int pass = 0;; ++pass) {
    f32x16 cm;
#pragma unroll
    for (int r = 0; r < 16; ++r) cm[r] = -m0;
    PIN(cm);
    O = f32x16{};
    f32x16 SA, SB = {};
    u32x4 kx = LDX(0), kf = LDK(0, 0), va, vb = {0, 0, 0, 0}, kxn;
    SA = mfma32(kx, q2[0], cm);
    SA = mfma32(kf, q1, SA);
    kf = LDK(0, 1);
    va = LDV(0, 0);
    PIN(SB); PIN(vb);
#define EX4(S, i) do { S[i] = __builtin_amdgcn_exp2f(S[i]); S[i + 1] = __builtin_amdgcn_exp2f(S[i + 1]); \
                       S[i + 2] = __builtin_amdgcn_exp2f(S[i + 2]); S[i + 3] = __builtin_amdgcn_exp2f(S[i + 3]); PIN(S); } while (0)
#define STEP(C, N, KXN, QN, RDVB, RDK, RDVA, EXTRA) do { \
      O = smf_hi(vb, N, O, idx); SBAR(); vb = RDVB; EXTRA; EX4(C, 0); SBAR(); \
      N = mfma32(KXN, QN, cm); EX4(C, 4); SBAR(); \
      N = mfma32(kf, q1, N); SBAR(); kf = RDK; EX4(C, 8); SBAR(); \
      O = smf_lo(va, C, O, idx); SBAR(); va = RDVA; EX4(C, 12); SBAR(); } while (0)
    for (int G = 0; G < 16 * KVG; G += KVG) {
      if (pass == 0) {
        if (G == 3 * KVG) {
          asm volatile("s_waitcnt vmcnt(0)\n\ts_barrier" ::: "memory");
          retile_load(w1, w2, u0, lane, rv0);
          if (has1) retile_load(w1, w2, u1, lane, rv1);
          SBAR();
        }
        if (G == 8 * KVG) {
          retile_store(ws, u0, lane, rv0);
          if (has1) retile_store(ws, u1, lane, rv1);
          SBAR();
        }
      }
      STEP(SA, SB, kx, q2[1], LDV(G, 1), LDK(G, 2), LDV(G, 2), (void)0);
      STEP(SB, SA, kx, q2[2], LDV(G, 3), LDK(G, 3), LDV(G, 4), (void)0);
      STEP(SA, SB, kx, q2[3], LDV(G, 5), LDK(G + KVG, 0), LDV(G, 6), kxn = LDX(G + KVG));
      STEP(SB, SA, kxn, q2[0], LDV(G, 7), LDK(G + KVG, 1), LDV(G + KVG, 0), (void)0);
      kx = kxn;
    }
    O = smf_hi(vb, SB, O, idx);
    bool bad = false;
#pragma unroll
    for (int r = 0; r < 10; ++r) bad |= !(fabsf(O[r]) < 1e37f);
    if (pass == 1 || !__any(bad)) break;
    m0 = -INFINITY;
    for (int G = 0; G < 16 * KVG; G += KVG) {
      const u32x4 x4 = LDX(G);
#pragma unroll
      for (int t = 0; t < 4; ++t) {
        f32x16 z = {};
        f32x16 s = mfma32(x4, q2[t], z);
        s = mfma32(LDK(G, t), q1, s);
#pragma unroll
        for (int r = 0; r < 16; ++r) m0 = fmaxf(m0, s[r]);
      }
    }
    m0 = fmaxf(m0, __shfl_xor(m0, 32));
  }
  const float rl = 1.0f / O[9];
  const int tok = 64 * g + 32 * (w & 1) + r32, hh = 2 * head + hi;
  char* ob = ws + O_OFF + (size_t)(tok >> 4) * 18 * 1024 + (tok & 15) * 16;
  u32x4 o = {pack2(O[0] * rl, O[1] * rl), pack2(O[2] * rl, O[3] * rl), pack2(O[4] * rl, O[5] * rl), pack2(O[6] * rl, O[7] * rl)};
  *(u32x4*)(ob + (hh >> 2) * 1024 + (hh & 3) * 256) = o;
  *(u16*)(ob + (16 + (hh >> 5)) * 1024 + ((hh & 31) >> 3) * 256 + (hh & 7) * 2) = f2bf(O[8] * rl);
#undef LDK
#undef LDX
#undef LDV
#undef EX4
#undef STEP
}

__global__ __launch_bounds__(256) void k_wo(const u16* __restrict__ x, char* __restrict__ ws) {
  __shared__ __attribute__((aligned(16))) char smem[90 * 1024];
  LAS char* L = (LAS char*)smem;
  const int tid = threadIdx.x, lane = tid & 63, w = __builtin_amdgcn_readfirstlane(tid >> 6);
  const int j = blockIdx.x & 7, g = blockIdx.x >> 3, g4 = lane >> 4;
  const int t0 = j < 4 ? 5 * j : 20 + 4 * (j - 4), nt = j < 4 ? 5 : 4;
  const unsigned lds0 = (unsigned)(uintptr_t)L;
#pragma unroll
  for (int i = 0; i < 23; ++i) {
    int f = w + 4 * i; f = f < 90 ? f : 89;
    glds16(ws + WO_OFF + (size_t)(t0 * 18 + f) * 1024 + lane * 16, lds0 + f * 1024);
  }
  const int mt = 4 * g + w, tok = 16 * mt + (lane & 15);
  u32x4 of[18];
#pragma unroll
  for (int s = 0; s < 18; ++s) of[s] = *(const u32x4*)(ws + O_OFF + (size_t)(mt * 18 + s) * 1024 + lane * 16);
  const float* mod = (const float*)(ws + MOD_OFF);
  u32x2 xv[5]; f32x4 ga[5];
#pragma unroll
  for (int t = 0; t < 5; ++t) {
    const int n = 16 * (t < nt ? t0 + t : t0) + 4 * g4;
    xv[t] = *(const u32x2*)(x + (size_t)tok * DM + n);
    ga[t] = *(const f32x4*)(mod + 2 * DM + n);
  }
  asm volatile("s_waitcnt vmcnt(0)\n\ts_barrier" ::: "memory");
  f32x4 acc[5];
#pragma unroll
  for (int t = 0; t < 5; ++t) acc[t] = f32x4{0, 0, 0, 0};
#pragma unroll
  for (int s = 0; s < 18; ++s)
#pragma unroll
    for (int t = 0; t < 5; ++t) acc[t] = mfma16(*(const LAS u32x4*)(L + (t * 18 + s) * 1024 + lane * 16), of[s], acc[t]);
#pragma unroll
  for (int t = 0; t < 5; ++t) {
    if (t < nt) {
      const int T = t0 + t;
      const float v0 = bflo(xv[t][0]) + rbf(ga[t][0] * rbf(acc[t][0])), v1 = bfhi(xv[t][0]) + rbf(ga[t][1] * rbf(acc[t][1]));
      const float v2 = bflo(xv[t][1]) + rbf(ga[t][2] * rbf(acc[t][2])), v3 = bfhi(xv[t][1]) + rbf(ga[t][3] * rbf(acc[t][3]));
      u32x2 o = {pack2(v0, v1), pack2(v2, v3)};
      *(u32x2*)(ws + X1_OFF + (size_t)(mt * 18 + (T >> 1)) * 1024 + (((T & 1) * 2 + (g4 >> 1)) * 16 + (lane & 15)) * 16 + (g4 & 1) * 8) = o;
    }
  }
}

constexpr int WIN = 27;
__global__ __launch_bounds__(256, 1) void k_ffn(char* __restrict__ ws) {
  __shared__ __attribute__((aligned(16))) char smem[2 * 72 * 1024 + 2 * DM * 4];
  LAS char* L = (LAS char*)smem;
  const int tid = threadIdx.x, lane = tid & 63, w = __builtin_amdgcn_readfirstlane(tid >> 6);
  const int j = blockIdx.x & 7, g = blockIdx.x >> 3;
  const u32x4* wp = (const u32x4*)(ws + W12_OFF + (size_t)(j * 4 + w) * 324 * 1024) + lane;
  {
    u32x4 xf[18];
#pragma unroll
    for (int s = 0; s < 18; ++s) xf[s] = *(const u32x4*)(ws + X1_OFF + (size_t)((4 * g + w) * 18 + s) * 1024 + lane * 16);
    const float* mod = (const float*)(ws + MOD_OFF);
    f32x4 mv = {0, 0, 0, 0}, mv2 = {0, 0, 0, 0};
    if (tid < 144) { mv = *(const f32x4*)(mod + 4 * DM + 4 * tid); mv2 = *(const f32x4*)(mod + 3 * DM + 4 * tid); }
    SBAR();
    u32x4 win[WIN];
#pragma unroll
    for (int i = 0; i < WIN; ++i) { win[i] = wp[i * 64]; SBAR(); }
    LAS float* cs = (LAS float*)(L + 144 * 1024);
    if (tid < 144) {
#pragma unroll
      for (int e = 0; e < 4; ++e) mv[e] = rbf(1.0f + mv[e]);
      *(LAS f32x4*)(cs + 4 * tid) = mv;
      *(LAS f32x4*)(cs + DM + 4 * tid) = mv2;
    }
    __syncthreads();
    ln_mod(xf, cs, lane);
#pragma unroll
    for (int s = 0; s < 18; ++s) *(LAS u32x4*)(L + (s * 4 + w) * 1024 + lane * 16) = xf[s];
    __syncthreads();
    for (int grp = 0; grp < 6; ++grp) {
      const LAS char* bp = L + (grp >= 3 ? 72 * 1024 : 0) + lane * 16;
      if (grp == 3) __syncthreads();
      f32x4 acc[3][4];
#pragma unroll
      for (int p = 0; p < 3; ++p)
#pragma unroll
        for (int m = 0; m < 4; ++m) acc[p][m] = f32x4{0, 0, 0, 0};
      u32x4 bf[2][4];
#pragma unroll
      for (int m = 0; m < 4; ++m) bf[0][m] = *(const LAS u32x4*)(bp + m * 1024);
      const u32x4* wn = wp + (size_t)(grp * 54 + WIN) * 64;
      const bool more = grp < 5;
      SBAR();
#pragma unroll
      for (int s = 0; s < 18; ++s) {
#pragma unroll
        for (int p = 0; p < 3; ++p) {
          const int i = s * 3 + p;
          if (p == 0 && s < 17) {
#pragma unroll
            for (int m = 0; m < 4; ++m) bf[(s + 1) & 1][m] = *(const LAS u32x4*)(bp + ((s + 1) * 4 + m) * 1024);
          }
#pragma unroll
          for (int m = 0; m < 4; ++m) acc[p][m] = mfma16(win[i % WIN], bf[s & 1][m], acc[p][m]);
          SBAR();
          win[i % WIN] = wn[(i + WIN < 54 || more ? i : i - WIN) * 64];
          SBAR();
        }
      }
      if (grp < 3) {
#pragma unroll
        for (int p = 0; p < 3; ++p) {
          const int T = 9 * w + 3 * grp + p;
#pragma unroll
          for (int m = 0; m < 4; ++m) {
            u32x2 o = {pack2(fmaxf(acc[p][m][0], 0.f), fmaxf(acc[p][m][1], 0.f)), pack2(fmaxf(acc[p][m][2], 0.f), fmaxf(acc[p][m][3], 0.f))};
            *(LAS u32x2*)(L + 72 * 1024 + ((T >> 1) * 4 + m) * 1024 + lane * 16 + (T & 1) * 8) = o;
          }
        }
      } else {
#pragma unroll
        for (int p = 0; p < 3; ++p) {
          const int T = 9 * w + 3 * (grp - 3) + p;
#pragma unroll
          for (int u = 0; u < 2; ++u) {
            const f32x4 a = acc[p][2 * u], b = acc[p][2 * u + 1];
            u32x4 o = {pack2(a[0], a[1]), pack2(a[2], a[3]), pack2(b[0], b[1]), pack2(b[2], b[3])};
            *(u32x4*)(ws + PART_OFF + (size_t)(((j * 32 + g) * 36 + T) * 2 + u) * 1024 + lane * 16) = o;
          }
        }
      }
    }
  }
}

__global__ __launch_bounds__(64) void k_out(const char* __restrict__ ws, u16* __restrict__ out) {
  const int lane = threadIdx.x, id = blockIdx.x;
  const int u = id & 1, blk = (id >> 1) % 9, g = (id >> 1) / 9, g4 = lane >> 4;
  u32x4 pv[8][4];
#pragma unroll
  for (int s = 0; s < 8; ++s)
#pragma unroll
    for (int q = 0; q < 4; ++q) pv[s][q] = *(const u32x4*)(ws + PART_OFF + (size_t)(((s * 32 + g) * 36 + 4 * blk + q) * 2 + u) * 1024 + lane * 16);
  const int n0 = 64 * blk + 16 * g4;
  const float* gb = (const float*)(ws + MOD_OFF) + 5 * DM + n0;
  f32x4 gv[4];
#pragma unroll
  for (int r = 0; r < 4; ++r) gv[r] = *(const f32x4*)(gb + 4 * r);
  u32x4 xv[2][2];
#pragma unroll
  for (int mm = 0; mm < 2; ++mm)
#pragma unroll
    for (int hh = 0; hh < 2; ++hh)
      xv[mm][hh] = *(const u32x4*)(ws + X1_OFF + (size_t)((4 * g + 2 * u + mm) * 18 + (n0 >> 5)) * 1024 + ((((n0 >> 3) & 3) + hh) * 16 + (lane & 15)) * 16);
#pragma unroll
  for (int mm = 0; mm < 2; ++mm) {
    float y[16];
#pragma unroll
    for (int q = 0; q < 4; ++q) {
      float a0 = 0, a1 = 0, a2 = 0, a3 = 0;
#pragma unroll
      for (int s = 0; s < 8; ++s) {
        a0 += bflo(pv[s][q][2 * mm]); a1 += bfhi(pv[s][q][2 * mm]); a2 += bflo(pv[s][q][2 * mm + 1]); a3 += bfhi(pv[s][q][2 * mm + 1]);
      }
      y[q] = a0; y[4 + q] = a1; y[8 + q] = a2; y[12 + q] = a3;
    }
    u32x4 o[2];
#pragma unroll
    for (int e = 0; e < 8; ++e) {
      const unsigned xx = xv[mm][e >> 2][e & 3];
      const float v0 = bflo(xx) + rbf(gv[e >> 1][(2 * e) & 3] * rbf(y[2 * e])), v1 = bfhi(xx) + rbf(gv[e >> 1][(2 * e + 1) & 3] * rbf(y[2 * e + 1]));
      o[e >> 2][e & 3] = pack2(v0, v1);
    }
    const int tok = 64 * g + 16 * (2 * u + mm) + (lane & 15);
    u32x4* op = (u32x4*)(out + (size_t)tok * DM + n0);
    op[0] = o[0]; op[1] = o[1];
  }
}

extern "C" void kernel_launch(void* const* d_in, const int* in_sizes, int n_in, void* d_out, int out_size, void* d_ws, size_t ws_size,
                              hipStream_t stream) {
  const u16* x = (const u16*)d_in[0];
  const u16* wq = (const u16*)d_in[1];
  const u16* wk = (const u16*)d_in[2];
  const u16* wv = (const u16*)d_in[3];
  const u16* wo = (const u16*)d_in[4];
  const u16* w1 = (const u16*)d_in[6];
  const u16* w2 = (const u16*)d_in[7];
  const u16* c = (const u16*)d_in[8];
  const u16* wmod = (const u16*)d_in[9];
  char* ws = (char*)d_ws;
  k_prep<<<K1_MOD + K1_QKV + K1_WO, 256, 0, stream>>>(c, wmod, wq, wk, wv, wo, ws);
  k_qkv<<<256, 256, 0, stream>>>(x, ws);
  k_attn<<<256, 512, 0, stream>>>(w1, w2, ws);
  k_wo<<<256, 256, 0, stream>>>(x, ws);
  k_ffn<<<256, 256, 0, stream>>>(ws);
  k_out<<<576, 64, 0, stream>>>(ws, (u16*)d_out);
}
```

```cpp
#include <hip/hip_runtime.h>
#include <stdint.h>

typedef unsigned short u16;
typedef __attribute__((ext_vector_type(8))) __bf16 bf16x8;
typedef __attribute__((ext_vector_type(16))) __bf16 bf16x16;
typedef __attribute__((ext_vector_type(2))) __bf16 bf16x2;
typedef __attribute__((ext_vector_type(2))) float f32x2;
typedef __attribute__((ext_vector_type(4))) float f32x4;
typedef __attribute__((ext_vector_type(8))) float f32x8;
typedef __attribute__((ext_vector_type(16))) float f32x16;
typedef __attribute__((ext_vector_type(4))) unsigned u32x4;
typedef __attribute__((ext_vector_type(2))) unsigned u32x2;
#define LAS __attribute__((address_space(3)))
#define SBAR() __builtin_amdgcn_sched_barrier(0)
#define PIN(x) asm volatile("" : "+v"(x))

constexpr int SEQ = 2048, DM = 576, FF = 4608;
constexpr size_t KB = 1024;
constexpr size_t MOD_OFF = 0;
constexpr size_t GRAN_OFF = 16 * KB;
constexpr size_t EPOCH_OFF = 24 * KB;
constexpr size_t CTRL_OFF = 32 * KB;
constexpr size_t KV_OFF = 64 * KB;
constexpr size_t GRP_OFF = 256 * KB;
constexpr int GRP = 152 * 1024;
constexpr int STAT_G = 0;
constexpr int OX_G = 8 * 1024;
constexpr int O_G = 16 * 1024;
constexpr int X1_G = 80 * 1024;
constexpr size_t W12_OFF = GRP_OFF + (size_t)32 * GRP;
constexpr size_t PART_OFF = W12_OFF + 8 * 1296 * KB;
constexpr size_t WS_END = PART_OFF + (size_t)32 * 64 * 9 * KB;
constexpr int KVG = 9728;
constexpr int C_W = 0, C_P = 8, C_F = 40, N_CTRL = 72;
#define GAS __attribute__((address_space(1)))
typedef GAS unsigned gu32;
#define RLX_AGENT __ATOMIC_RELAXED, __HIP_MEMORY_SCOPE_AGENT
constexpr int SC1 = 16;

template <int AUX> __device__ __forceinline__ void st128_(u32x4 v, __amdgpu_buffer_rsrc_t r, int voff, int soff) {
  PIN(v);
  __builtin_amdgcn_raw_buffer_store_b128(v, r, voff, soff, AUX);
  asm volatile("s_nop 1" ::"v"(v) : "memory");
}
#define st128(v, r, voff, soff, aux) st128_<aux>(v, r, voff, soff)
__device__ __forceinline__ float bf2f(u16 v) { return __uint_as_float((unsigned)v << 16); }
__device__ __forceinline__ float bflo(unsigned v) { return __uint_as_float(v << 16); }
__device__ __forceinline__ float bfhi(unsigned v) { return __uint_as_float(v & 0xffff0000u); }
__device__ __forceinline__ unsigned pack2(float lo, float hi) {
  f32x2 v = {lo, hi};
  return __builtin_bit_cast(unsigned, __builtin_convertvector(v, bf16x2));
}
typedef __attribute__((ext_vector_type(2))) short s16x2;
__device__ __forceinline__ unsigned relu2(unsigned v) {
  return __builtin_bit_cast(unsigned, __builtin_elementwise_max(__builtin_bit_cast(s16x2, v), s16x2{0, 0}));
}
__device__ __forceinline__ float rbf(float v) { return (float)(__bf16)v; }
__device__ __forceinline__ u16 f2bf(float v) { return __builtin_bit_cast(u16, (__bf16)v); }
__device__ __forceinline__ f32x4 mfma16(u32x4 a, u32x4 b, f32x4 c) {
  return __builtin_amdgcn_mfma_f32_16x16x32_bf16(__builtin_bit_cast(bf16x8, a), __builtin_bit_cast(bf16x8, b), c, 0, 0, 0);
}
__device__ __forceinline__ f32x16 mfma32(u32x4 a, u32x4 b, f32x16 c) {
  return __builtin_amdgcn_mfma_f32_32x32x16_bf16(__builtin_bit_cast(bf16x8, a), __builtin_bit_cast(bf16x8, b), c, 0, 0, 0);
}
__device__ __forceinline__ f32x16 smf_lo(u32x4 a, f32x16 p, f32x16 c, int idx) {
  f32x8 h = __builtin_shufflevector(p, p, 0, 1, 2, 3, 4, 5, 6, 7);
  return __builtin_amdgcn_smfmac_f32_32x32x32_bf16(__builtin_bit_cast(bf16x8, a), __builtin_bit_cast(bf16x16, h), c, idx, 0, 0);
}
__device__ __forceinline__ f32x16 smf_hi(u32x4 a, f32x16 p, f32x16 c, int idx) {
  f32x8 h = __builtin_shufflevector(p, p, 8, 9, 10, 11, 12, 13, 14, 15);
  return __builtin_amdgcn_smfmac_f32_32x32x32_bf16(__builtin_bit_cast(bf16x8, a), __builtin_bit_cast(bf16x16, h), c, idx, 0, 0);
}
__device__ __forceinline__ void glds16s(const void* sbase, unsigned voff, unsigned lds_base) {
  unsigned sv;
  asm volatile("s_mov_b32 %0, m0\n\ts_mov_b32 m0, %3\n\ts_nop 0\n\tglobal_load_lds_dwordx4 %1, %2\n\ts_mov_b32 m0, %0"
               : "=&s"(sv) : "v"(voff), "s"(sbase), "s"(lds_base) : "memory");
}
__device__ __forceinline__ int vpos(int d) {
  return d < 4 ? d : d < 12 ? d + 7 : d < 16 ? d - 8 : d == 16 ? 19 : 8;
}

constexpr int MSPLIT = 4;
constexpr int WIN = 17;
constexpr int WQ0 = 14;
constexpr int WIN0 = 12;
constexpr int OWN_LDS = 32 * 1024;
constexpr int XO_LDS = 144 * 1024, CS_LDS = XO_LDS + 8 * DM * 2, MAIN_LDS = CS_LDS + 3 * DM * 4;
constexpr int WI_LDS = 24 * 1024, RED_LDS = WI_LDS + 72 * 1408;
constexpr int WQ_LDS = 72 * 1024, KW_LDS = 40 * 1024, QS_LDS = 60 * 1024, VS_LDS = 69 * 1024;
constexpr int CS1_LDS = CS_LDS, GR_LDS = CS_LDS + 2 * DM * 4, TK_LDS = GR_LDS + 1024, KX_LDS = QS_LDS;
static_assert(WQ_LDS + 81 * 1024 <= CS_LDS, "");
typedef __attribute__((ext_vector_type(4))) short s16x4;
typedef __amdgpu_buffer_rsrc_t rsrc_t;
__device__ __forceinline__ int run_base(int T) {
  return T < 20 ? (T / 5) * 180 + T % 5 : 720 + ((T - 20) >> 2) * 144 + ((T - 20) & 3);
}
struct Retile {
  rsrc_t src; int voff, s1, s4, vst, dst[4];
  __device__ __forceinline__ void init(const u16* w1, const u16* w2, int slice, int u, bool valid, int lane) {
    asm volatile("" : "+s"(u), "+v"(lane));
    const bool second = u >= 162;
    const int uu = second ? u - 162 : u, ks = uu / 9, blk = uu - 9 * ks;
    const char* base = second ? (const char*)(w2 + (size_t)(576 * slice + 64 * (ks >> 1) + 2 * (ks & 1)) * DM + 64 * blk)
                              : (const char*)(w1 + (size_t)(32 * ks) * FF + 576 * slice + 64 * blk);
    src = __builtin_amdgcn_make_buffer_rsrc((void*)base, 0, valid ? (second ? 62 * DM * 2 : 31 * FF * 2 + 128) : 0, 0x00020000);
    voff = (lane >> 4) * (second ? 16 * DM * 2 : 8 * FF * 2) + (lane & 15) * 8;
    s1 = second ? 4 * DM * 2 : FF * 2; s4 = second ? DM * 2 : 4 * FF * 2;
#pragma unroll
    for (int q = 0; q < 4; ++q) {
      dst[q] = (int)W12_OFF + (slice * 1296 + run_base(4 * blk + q) + ((second ? 18 : 0) + ks) * (blk < 5 ? 5 : 4)) * 1024;
      asm volatile("" : "+s"(dst[q]));
    }
    vst = valid ? lane * 16 : 0x7fffffff;
  }
  __device__ __forceinline__ u32x2 load(int e) const { return __builtin_amdgcn_raw_buffer_load_b64(src, voff, (e & 3) * s1 + (e >> 2) * s4, 0); }
  __device__ __forceinline__ void store(rsrc_t rs, int q, const u32x2 (&v)[8]) const {
    u32x4 o;
#pragma unroll
    for (int e = 0; e < 4; ++e)
      o[e] = __builtin_amdgcn_perm(v[2 * e + 1][q >> 1], v[2 * e][q >> 1], (q & 1) ? 0x07060302u : 0x05040100u);
    st128(o, rs, vst, dst[q], SC1);
  }
};
__device__ __forceinline__ void make_q2(u32x2 (&q2)[2], unsigned qx, float m, int hi) {
  const float nm = -m;
  const unsigned h = __float_as_uint(nm) & 0xffff0000u;
  const float r1 = nm - __uint_as_float(h);
  const unsigned mid = __float_as_uint(r1) & 0xffff0000u;
  const unsigned lo = __float_as_uint(r1 - __uint_as_float(mid)) >> 16;
  const unsigned d0 = (h >> 16) | mid;
  q2[0] = u32x2{hi ? d0 : qx, hi ? lo : 0u};
  q2[1] = u32x2{hi ? d0 : 0u, hi ? lo : qx};
  PIN(q2[0]); PIN(q2[1]);
}
template <int T> __device__ __forceinline__ f32x16 mfma_x(u32x4 kx, const u32x2 (&q2)[2]) {
  const u32x2 a = (T & 2) ? __builtin_shufflevector(kx, kx, 2, 3) : __builtin_shufflevector(kx, kx, 0, 1);
  return __builtin_amdgcn_mfma_f32_32x32x8bf16_1k(__builtin_bit_cast(s16x4, a), __builtin_bit_cast(s16x4, q2[T & 1]), f32x16{}, 0, 0, 0);
}
__device__ __forceinline__ float rowmax16(const f32x16& s, float m) {
#pragma unroll
  for (int r = 0; r < 16; ++r) m = fmaxf(m, s[r]);
  return m;
}
#define BAR_LGKM() do { SBAR(); asm volatile("s_waitcnt lgkmcnt(0)\n\ts_barrier" ::: "memory"); SBAR(); } while (0)
#define DRAIN_BAR() do { SBAR(); asm volatile("s_waitcnt vmcnt(0) lgkmcnt(0)\n\ts_barrier" ::: "memory"); SBAR(); } while (0)
__device__ __forceinline__ void poll_ge(gu32* word, unsigned want) {
  for (unsigned spins = 0; spins < (1u << 16); ++spins)
    if (__hip_atomic_load(word, RLX_AGENT) >= want) break;
  __builtin_amdgcn_fence(__ATOMIC_ACQUIRE, "wavefront");
}

constexpr unsigned EMPTY = 0xFFFFFFFFu;
constexpr unsigned SPIN_CAP = 1u << 15;
__device__ __forceinline__ unsigned san(unsigned v) { return v < EMPTY - 1u ? v : EMPTY - 1u; }
__device__ __forceinline__ u32x2 san(u32x2 v) { return u32x2{san(v[0]), san(v[1])}; }
__device__ __forceinline__ u32x4 san(u32x4 v) { return u32x4{san(v[0]), san(v[1]), san(v[2]), san(v[3])}; }
__device__ __forceinline__ unsigned umax4(unsigned m, u32x4 v) { return max(max(max(m, v[0]), v[1]), max(v[2], v[3])); }
#define FULL(m) __all((m) != EMPTY)
template <int N> __device__ __forceinline__ void until_full(u32x4 (&v)[N], int n, __amdgpu_buffer_rsrc_t rs, int voff, int soff, int step) {
  unsigned m = 0;
#pragma unroll
  for (int i = 0; i < N; ++i) if (i < n) m = umax4(m, v[i]);
  if (__builtin_expect(!FULL(m), 0)) {
#pragma unroll
    for (int i = 0; i < N; ++i)
      if (i < n) {
#pragma unroll 1
        for (unsigned spins = 0; spins < SPIN_CAP && !FULL(umax4(0u, v[i])); ++spins)
          asm volatile("buffer_load_dwordx4 %0, %1, %2, %3 offen sc1\n\ts_waitcnt vmcnt(0)" : "+v"(v[i]) : "v"(voff), "s"(rs), "s"(soff + i * step) : "memory");
      }
  }
}
template <int CTRL> __device__ __forceinline__ float dpp_add(float v) {
  return v + __int_as_float(__builtin_amdgcn_update_dpp(0, __float_as_int(v), CTRL, 0xf, 0xf, true));
}
__device__ __forceinline__ float xsum32(float v) {
  const auto r = __builtin_amdgcn_permlane32_swap(__float_as_uint(v), __float_as_uint(v), false, false);
  float o = __uint_as_float(r[0]) + __uint_as_float(r[1]);
  PIN(o);
  return o;
}
__device__ __forceinline__ float xmax32(float v) {
  const auto r = __builtin_amdgcn_permlane32_swap(__float_as_uint(v), __float_as_uint(v), false, false);
  float o = fmaxf(__uint_as_float(r[0]), __uint_as_float(r[1]));
  PIN(o);
  return o;
}
__device__ __forceinline__ float xsum_g4(float v) {
  const auto r = __builtin_amdgcn_permlane16_swap(__float_as_uint(v), __float_as_uint(v), false, false);
  return xsum32(__uint_as_float(r[0]) + __uint_as_float(r[1]));
}
typedef __attribute__((ext_vector_type(4))) short v4i16;
template <int SECOND = 640>
__device__ __forceinline__ u32x4 tr_frag(const LAS char* p) {
  const u32x2 lo = __builtin_bit_cast(u32x2, __builtin_amdgcn_ds_read_tr16_b64_v4i16((LAS v4i16*)p));
  const u32x2 hi = __builtin_bit_cast(u32x2, __builtin_amdgcn_ds_read_tr16_b64_v4i16((LAS v4i16*)(p + SECOND)));
  return u32x4{lo[0], lo[1], hi[0], hi[1]};
}
__device__ __forceinline__ void load_wfr(u32x4 (&wfr)[9][5], const LAS char* L, int lane, int kh) {
  const LAS char* wp = L + WI_LDS + kh * 9 * 5632 + (lane >> 4) * 1408 + ((lane & 15) >> 2) * 160 + (lane & 3) * 8;
  const LAS char* wa = wp + kh * 64;
  const LAS char* wb = wp + 64 - kh * 64;
#pragma unroll
  for (int s = 0; s < 9; ++s) {
    wfr[s][0] = tr_frag(wa + s * 5632); wfr[s][1] = tr_frag(wa + s * 5632 + 32); wfr[s][2] = tr_frag(wp + s * 5632 + 128);
    wfr[s][3] = tr_frag(wb + s * 5632); wfr[s][4] = tr_frag(wb + s * 5632 + 32);
  }
}
__device__ __forceinline__ void small_gemm(f32x4 (&acc)[5], const u32x4 (&wfr)[9][5], const u32x4 (&bf)[9], LAS char* L, int l16, int mw, int kh) {
#pragma unroll
  for (int t = 0; t < 5; ++t) acc[t] = f32x4{0, 0, 0, 0};
#pragma unroll
  for (int s = 0; s < 9; ++s)
#pragma unroll
    for (int t = 0; t < 5; ++t) acc[t] = mfma16(wfr[s][t], bf[s], acc[t]);
  LAS char* rp = L + RED_LDS + mw * 5120 + l16;
  *(LAS f32x4*)(rp + (2 - 2 * kh) * 1024) = acc[3];
  *(LAS f32x4*)(rp + (3 - 2 * kh) * 1024) = acc[4];
  if (kh) *(LAS f32x4*)(rp + 4 * 1024) = acc[2];
  BAR_LGKM();
  acc[0] += *(const LAS f32x4*)(rp + (2 * kh) * 1024);
  acc[1] += *(const LAS f32x4*)(rp + (2 * kh + 1) * 1024);
  acc[2] += *(const LAS f32x4*)(rp + 4 * 1024);
}

template <int P>
__device__ __forceinline__ void ffn_phase(LAS char* L, rsrc_t wr, rsrc_t rs, u32x4 (&win)[WIN], int T0, int lane, int pbase, int jown) {
  const int l16 = lane * 16;
  constexpr int NF = 36 * P;
#pragma unroll
  for (int ph = 0; ph < 2; ++ph) {
    const LAS char* bp = L + ph * 72 * 1024 + l16;
    f32x4 acc[P][4];
#pragma unroll
    for (int p = 0; p < P; ++p)
#pragma unroll
      for (int m = 0; m < 4; ++m) acc[p][m] = f32x4{0, 0, 0, 0};
    u32x4 bf[2][4];
#pragma unroll
    for (int m = 0; m < 4; ++m) bf[0][m] = *(const LAS u32x4*)(bp + m * 1024);
    SBAR();
#pragma unroll
    for (int s = 0; s < 18; ++s) {
#pragma unroll
      for (int p = 0; p < P; ++p) {
        const int i = (ph * 18 + s) * P + p;
        if (p < 4 && s < 17) bf[(s + 1) & 1][p] = *(const LAS u32x4*)(bp + ((s + 1) * 4 + p) * 1024);
#pragma unroll
        for (int m = 0; m < 4; ++m) acc[p][m] = mfma16(win[i % WIN], bf[s & 1][m], acc[p][m]);
        SBAR();
        if (i < WIN - WIN0) {
          win[(WIN0 + 2 * i) % WIN] = __builtin_amdgcn_raw_buffer_load_b128(wr, l16, (WIN0 + 2 * i) * 1024, SC1);
          SBAR();
          win[(WIN0 + 2 * i + 1) % WIN] = __builtin_amdgcn_raw_buffer_load_b128(wr, l16, (WIN0 + 2 * i + 1) * 1024, SC1);
        } else if (i + WIN < NF) win[i % WIN] = __builtin_amdgcn_raw_buffer_load_b128(wr, l16, (i + WIN) * 1024, SC1);
        SBAR();
      }
    }
    if (ph == 0) {
#pragma unroll
      for (int p = 0; p < P; ++p) {
        const int T = T0 + p;
#pragma unroll
        for (int m = 0; m < 4; ++m) {
          u32x2 o = {relu2(pack2(acc[p][m][0], acc[p][m][1])), relu2(pack2(acc[p][m][2], acc[p][m][3]))};
          *(LAS u32x2*)(L + 72 * 1024 + ((T >> 1) * 4 + m) * 1024 + l16 + (T & 1) * 8) = o;
        }
      }
      BAR_LGKM();
    } else {
      const int vin = ((lane >> 4) * 4 + (lane & 3)) * 16, voff = ((lane >> 2) & 3) * 8 * 9216 + vin;
      const bool mine = ((lane >> 2) & 3) == (jown & 3);
      const int vo[2] = {mine && jown < 4 ? 0x7fffffff : voff, mine && jown >= 4 ? 0x7fffffff : voff};
#pragma unroll
      for (int p = 0; p < P; ++p)
#pragma unroll
        for (int u = 0; u < 2; ++u) {
          const f32x4 a = acc[p][2 * u], b = acc[p][2 * u + 1];
          u32x4 o = {pack2(a[0], a[1]), pack2(a[2], a[3]), pack2(b[0], b[1]), pack2(b[2], b[3])};
          st128(o, rs, vo[u], pbase + u * 4 * 8 * 9216 + (T0 + p) * 256, SC1);
          if (u == (jown >> 2) && mine) *(LAS u32x4*)(L + OWN_LDS + (T0 + p) * 256 + vin) = o;
        }
    }
  }
}

__global__ __launch_bounds__(512, 2) void k_main(const u16* __restrict__ x, const u16* __restrict__ wq, const u16* __restrict__ wk,
                                                 const u16* __restrict__ wv, const u16* __restrict__ wo, const u16* __restrict__ w1,
                                                 const u16* __restrict__ w2, const u16* __restrict__ cvec, const u16* __restrict__ wmod,
                                                 char* __restrict__ ws, u16* __restrict__ out) {
  __shared__ __attribute__((aligned(16))) char smem[MAIN_LDS];
  LAS char* L = (LAS char*)smem;
  const int w = __builtin_amdgcn_readfirstlane(threadIdx.x >> 6);
  const int xcd = blockIdx.x & 7, slot = blockIdx.x >> 3;
  const int j = (xcd / MSPLIT) * MSPLIT + slot % MSPLIT, g = (xcd % MSPLIT) * (32 / MSPLIT) + slot / MSPLIT;
  const int gb0 = (int)GRP_OFF + g * GRP;
  const int kh = w >> 2, mw = w & 3;
#define IDS() int tid = threadIdx.x; PIN(tid); const int lane = tid & 63, hi = lane >> 5, r32 = lane & 31, g4 = lane >> 4, l16 = lane * 16; \
    (void)hi; (void)r32; (void)g4; (void)l16
  const rsrc_t rs = __builtin_amdgcn_make_buffer_rsrc(ws, 0, (int)WS_END, 0x00020000);
  gu32* ctrl = (gu32*)(ws + CTRL_OFF);
  const int t0 = j < 4 ? 5 * j : 20 + 4 * (j - 4);
  unsigned epoch1;
  {
    IDS();
    unsigned epoch = 0;
#define TAG() (epoch * 2654435761u + 0x9E3779B9u)
    if (tid == 0) *(LAS unsigned*)(L + TK_LDS) = 0u;
    const int t2 = tid & 255, r3 = t2 < 64 ? t2 + 512 : t2;
    const u16* xp = x + (size_t)(64 * g + 8 * w + (lane >> 3)) * DM + 8 * (lane & 7);
    u32x4 xf[9], kw[3];
    int dv[3];
    auto wq_setup = [&]() {
#pragma unroll
      for (int k = 0; k < 3; ++k) {
        const int q = (k < 2 ? mw + 4 * k : 8) * 64 + lane, R = q / 9, row = (R & ~7) | ((R & 1) << 2) | ((R >> 1) & 3);
        dv[k] = row * (DM * 2) + (q - 9 * R) * 16;
        PIN(dv[k]);
      }
    };
    auto wq_dma = [&](int i) {
      const int k = i < 18 ? (i & 1) : 2, rot = g % 9;
      int a = i < 18 ? (i >> 1) + rot : mw + 4 * (i - 18);
      a = a >= 9 ? (i < 18 ? a - 9 : 8) : a;
      glds16s((const char*)wq + 144 * j + a * (64 * DM * 2), dv[k], (unsigned)(uintptr_t)L + WQ_LDS + (a * 9 + (k < 2 ? mw + 4 * k : 8)) * 1024);
    };
    const int tok32 = (mw & 1) * 16 + (lane & 15);
    int qoff[6];
    auto q_offsets = [&]() {
      int gq = g4; PIN(gq);
#pragma unroll
      for (int i = 0; i < 6; ++i) {
        const int a = i >> 1, c = (a < 2 ? 16 * (2 * kh + a) + 4 * gq : 64 + 4 * (gq & 1)) + 2 * (i & 1), hd = c / 18, d = c - 18 * hd, qt = hd * 2 + (mw >> 1);
        qoff[i] = QS_LDS + (d < 16 ? qt * 1024 + ((d >> 3) * 32 + tok32) * 16 + (d & 7) * 2 : 8192 + qt * 128 + tok32 * 4);
        PIN(qoff[i]);
      }
      SBAR();
    };
    if (kh == 0) {
      const int lg = (slot >> 2) * 8 + xcd, col = (lg < 54 ? lg * 4 + (slot & 3) : 0) * 16, jj = j % 3;
      const u32x4 empty = {EMPTY, EMPTY, EMPTY, EMPTY};
      if (blockIdx.x == 0 && t2 < N_CTRL) __hip_atomic_store(ctrl + 64 * t2, 0u, RLX_AGENT);
      if (lg < 18 && (lg * 4 + (slot & 3)) * 4 + mw < 152) st128(empty, rs, l16, (int)KV_OFF + ((lg * 4 + (slot & 3)) * 4 + mw) * 1024, SC1);
      SBAR();
      u32x4 wm[5]; u16 cv[5];
#pragma unroll
      for (int i = 0; i < 5; ++i) {
        int row = (t2 >> 1) + 128 * i; row = row < DM ? row : row - 128;
        wm[i] = *(const u32x4*)(wmod + (size_t)row * 3456 + col + 8 * (t2 & 1));
        cv[i] = cvec[row];
      }
      epoch = __hip_atomic_load((gu32*)(ws + EPOCH_OFF), RLX_AGENT);
      SBAR();
#pragma unroll
      for (int s = 0; s < 9; ++s) xf[s] = *(const u32x4*)(xp + 64 * s);
      const rsrc_t kvr = __builtin_amdgcn_make_buffer_rsrc((void*)(j < 3 ? wk : wv), 0, DM * 36, 0x00020000);
      kw[0] = __builtin_amdgcn_raw_buffer_load_b128(kvr, t2 * 36, jj == 2 ? 20 : 16 * jj, 0);
      kw[1] = __builtin_amdgcn_raw_buffer_load_b128(kvr, (t2 + 256) * 36, jj == 2 ? 20 : 16 * jj, 0);
      kw[2] = __builtin_amdgcn_raw_buffer_load_b128(kvr, r3 * 36, jj == 2 ? 20 : 16 * jj, 0);
      SBAR();
      BAR_LGKM();
      q_offsets();
      float acc[8] = {0, 0, 0, 0, 0, 0, 0, 0};
#pragma unroll
      for (int i = 0; i < 5; ++i) {
        const float cc = bf2f(cv[i]);
        float sv = rbf(cc * __builtin_amdgcn_rcpf(1.0f + __expf(-cc)));
        if (i == 4) sv *= t2 < 128 ? 1.f : 0.f;
#pragma unroll
        for (int e = 0; e < 4; ++e) { acc[2 * e] += sv * bflo(wm[i][e]); acc[2 * e + 1] += sv * bfhi(wm[i][e]); }
      }
#pragma unroll
      for (int e = 0; e < 8; ++e) { acc[e] = dpp_add<0x128>(dpp_add<0x124>(dpp_add<0x122>(acc[e]))); PIN(acc[e]); }
      LAS float* gw = (LAS float*)(L + GR_LDS) + (mw * 4 + g4) * 16 + (lane & 1) * 8;
      if ((lane & 15) < 2) { *(LAS f32x4*)gw = f32x4{acc[0], acc[1], acc[2], acc[3]}; *(LAS f32x4*)(gw + 4) = f32x4{acc[4], acc[5], acc[6], acc[7]}; }
#pragma unroll
      for (int s = 0; s < 9; ++s) PIN(xf[s]);
      PIN(kw[0]); PIN(kw[1]); PIN(kw[2]); PIN(epoch);
      asm volatile("s_waitcnt vmcnt(0) lgkmcnt(0)" ::: "memory");
      unsigned tk = 0;
      if (lane == 0) tk = __hip_atomic_fetch_add((LAS unsigned*)(L + TK_LDS), 1u, __ATOMIC_RELAXED, __HIP_MEMORY_SCOPE_WORKGROUP);
      if (__builtin_amdgcn_readfirstlane(tk) == 3u && lg < 54) {
        asm volatile("" : "+v"(epoch)::"memory");
        const LAS float* gr = (const LAS float*)(L + GR_LDS) + (lane & 3) * 64 + (lane >> 2);
        float v = rbf(dpp_add<0x4E>(dpp_add<0xB1>((gr[0] + gr[16]) + (gr[32] + gr[48]))));
        const int n = col + (lane >> 2), ch = col / DM;
        if (ch == 1 || ch == 4) v = rbf(1.0f + v);
        float vn = __int_as_float(__builtin_amdgcn_update_dpp(0, __float_as_int(v), 0x104, 0xf, 0xf, true));
        PIN(v); PIN(vn);
        if (col < 2 * DM) {
          if ((lane & 7) == 0)
            __hip_atomic_store((GAS unsigned long long*)(ws + GRAN_OFF) + (n >> 1), ((unsigned long long)TAG() << 32) | pack2(v, vn), RLX_AGENT);
        } else if ((lane & 3) == 0) __builtin_amdgcn_raw_buffer_store_b32(__float_as_uint(v), rs, n * 4, (int)MOD_OFF, SC1);
      }
      SBAR();
    } else {
#pragma unroll
      for (int s = 0; s < 9; ++s) asm volatile("global_load_dwordx4 %0, %1, off offset:%2" : "=v"(xf[s]) : "v"(xp), "i"(128 * s) : "memory");
      wq_setup();
      q_offsets();
      BAR_LGKM();
#pragma unroll
      for (int i = 0; i < WQ0; ++i) wq_dma(i);
      asm volatile("s_waitcnt vmcnt(%9)" : "+v"(xf[0]), "+v"(xf[1]), "+v"(xf[2]), "+v"(xf[3]), "+v"(xf[4]), "+v"(xf[5]), "+v"(xf[6]), "+v"(xf[7]), "+v"(xf[8])
                   : "i"(WQ0) : "memory");
    }
    u32x4 ga0, ga1;
    {
      f32x2 xh[36], sa = {0, 0}, sb = {0, 0};
#pragma unroll
      for (int i = 0; i < 36; ++i) xh[i] = f32x2{bflo(xf[i >> 2][i & 3]), bfhi(xf[i >> 2][i & 3])};
#pragma unroll
      for (int i = 0; i < 36; i += 2) { sa += xh[i]; sb += xh[i + 1]; }
      const float mu = dpp_add<0x141>(dpp_add<0x4E>(dpp_add<0xB1>((sa[0] + sa[1]) + (sb[0] + sb[1])))) * (1.0f / DM);
      sa = f32x2{0, 0}; sb = f32x2{0, 0};
#pragma unroll
      for (int i = 0; i < 36; i += 2) {
        xh[i] -= f32x2{mu, mu}; xh[i + 1] -= f32x2{mu, mu};
        sa = __builtin_elementwise_fma(xh[i], xh[i], sa); sb = __builtin_elementwise_fma(xh[i + 1], xh[i + 1], sb);
      }
      const float r = rsqrtf(dpp_add<0x141>(dpp_add<0x4E>(dpp_add<0xB1>((sa[0] + sa[1]) + (sb[0] + sb[1])))) * (1.0f / DM) + 1e-5f);
      LAS char* xw = L + (((lane >> 2) & 1) * 4 + (w >> 1)) * 1024 + ((lane & 3) * 16 + (((w & 1) * 8 + (lane >> 3)) ^ (lane & 7))) * 16;
#pragma unroll
      for (int i = 0; i < 9; ++i) {
        u32x4 o;
#pragma unroll
        for (int e = 0; e < 4; ++e) { const f32x2 v = xh[4 * i + e] * f32x2{r, r}; o[e] = pack2(v[0], v[1]); }
        *(LAS u32x4*)(xw + i * 8192) = o;
      }
      BAR_LGKM();
      if (kh == 0)
        asm volatile("buffer_load_dwordx4 %0, %2, %4, %5 offen sc1\n\tbuffer_load_dwordx4 %1, %3, %4, %5 offen sc1"
                     : "=&v"(ga0), "=&v"(ga1) : "v"(t2 * 16), "v"(4096 + r32 * 16), "s"(rs), "s"((int)GRAN_OFF) : "memory");
#pragma unroll
      for (int s = 0; s < 9; ++s) xf[s] = *(const LAS u32x4*)(L + ((9 * kh + s) * 4 + mw) * 1024 + (lane ^ ((((kh + s) & 1) * 4) + g4)) * 16);
      BAR_LGKM();
#pragma unroll
      for (int s = 0; s < 9; ++s) PIN(xf[s]);
    }
    if (kh == 0) {
      *(LAS u32x4*)(L + KW_LDS + t2 * 16) = kw[0]; *(LAS u32x4*)(L + KW_LDS + 4096 + t2 * 16) = kw[1]; *(LAS u32x4*)(L + KW_LDS + r3 * 16) = kw[2];
      PIN(epoch);
      const unsigned tag = TAG();
      asm volatile("s_waitcnt vmcnt(0)" : "+v"(ga0), "+v"(ga1)::"memory");
#pragma unroll 1
      for (unsigned spins = 0; spins < (1u << 16) && !__all(ga0[1] == tag && ga0[3] == tag && ga1[1] == tag && ga1[3] == tag); ++spins)
        asm volatile("buffer_load_dwordx4 %0, %2, %4, %5 offen sc1\n\tbuffer_load_dwordx4 %1, %3, %4, %5 offen sc1\n\ts_waitcnt vmcnt(0)"
                     : "=&v"(ga0), "=&v"(ga1) : "v"(t2 * 16), "v"(4096 + r32 * 16), "s"(rs), "s"((int)GRAN_OFF) : "memory");
      *(LAS f32x4*)(L + CS1_LDS + t2 * 16) = f32x4{bflo(ga0[0]), bfhi(ga0[0]), bflo(ga0[2]), bfhi(ga0[2])};
      *(LAS f32x4*)(L + CS1_LDS + 4096 + r32 * 16) = f32x4{bflo(ga1[0]), bfhi(ga1[0]), bflo(ga1[2]), bfhi(ga1[2])};
    } else {
#pragma unroll
      for (int i = WQ0; i < 21; ++i) wq_dma(i);
    }
    BAR_LGKM();
    f32x4 akv;
    {
      f32x4 a1 = {0, 0, 0, 0}, a2 = {0, 0, 0, 0};
      const LAS char* kwp = L + KW_LDS + (288 * kh + 8 * g4 + ((lane & 15) >> 2)) * 16 + (lane & 1) * 8;
      u32x4 f[9];
      const LAS float* cp = (const LAS float*)(L + CS1_LDS) + 288 * kh + 8 * g4;
      f32x4 cs[9][4];
#pragma unroll
      for (int s = 0; s < 9; ++s) {
        cs[s][0] = *(const LAS f32x4*)(cp + DM + 32 * s); cs[s][1] = *(const LAS f32x4*)(cp + 32 * s);
        cs[s][2] = *(const LAS f32x4*)(cp + DM + 32 * s + 4); cs[s][3] = *(const LAS f32x4*)(cp + 32 * s + 4);
        f[s] = tr_frag<64>(kwp + s * 512);
      }
      SBAR();
#pragma unroll
      for (int s = 0; s < 9; ++s) {
        const f32x4 a = {bflo(xf[s][0]), bfhi(xf[s][0]), bflo(xf[s][1]), bfhi(xf[s][1])}, b = {bflo(xf[s][2]), bfhi(xf[s][2]), bflo(xf[s][3]), bfhi(xf[s][3])};
        const f32x4 ha = __builtin_elementwise_fma(a, cs[s][0], cs[s][1]), hb = __builtin_elementwise_fma(b, cs[s][2], cs[s][3]);
        xf[s] = u32x4{pack2(ha[0], ha[1]), pack2(ha[2], ha[3]), pack2(hb[0], hb[1]), pack2(hb[2], hb[3])};
        PIN(xf[s]);
        if (s & 1) a2 = mfma16(f[s], xf[s], a2); else a1 = mfma16(f[s], xf[s], a1);
        SBAR();
      }
      akv = a1 + a2;
      if (kh) *(LAS f32x4*)(L + KX_LDS + mw * 1024 + l16) = akv;
    }
    if (kh) asm volatile("s_waitcnt vmcnt(0)" ::: "memory");
    BAR_LGKM();
    {
      f32x4 a = akv;
      if (kh == 0) {
        a += *(const LAS f32x4*)(L + KX_LDS + mw * 1024 + l16);
        const int kvb = (int)KV_OFF + (g >> 1) * KVG, tile = 2 * (g & 1) + (mw >> 1);
        if (j < 2) {
          if (hi) __builtin_amdgcn_raw_buffer_store_b64(san(u32x2{pack2(a[0], a[1]), pack2(a[2], a[3])}), rs, tok32 * 16 + (g4 & 1) * 8, kvb + tile * 1024 + j * 512, SC1);
        } else if (j == 2) {
          if (g4 == 3) __builtin_amdgcn_raw_buffer_store_b32(san(pack2(a[2], a[3])), rs, tok32 * 16, kvb + 4096 + tile * 4, SC1);
        } else if (j < 6) {
          LAS char* vs = L + VS_LDS + mw * 256;
          if (hi) {
#pragma unroll
            for (int r = 0; r < 4; ++r) *(LAS u16*)(vs + ((g4 & 1) * 4 + r) * 32 + (lane & 15) * 2) = f2bf(a[r]);
          }
          asm volatile("s_waitcnt lgkmcnt(0)" ::: "memory");
          const u32x4 vv = *(const LAS u32x4*)(vs + (lane & 15) * 16);
          const int d = (j == 5 ? 10 : 8 * (j - 3)) + ((lane & 15) >> 1);
          if (lane < 16 && (j < 5 || d >= 16))
            st128(san(vv), rs, (lane & 1) * 320 + vpos(d) * 16, kvb + 4608 + (4 * (g & 1) + mw) * 640, SC1);
        } else {
          const unsigned cw = j == 7 ? 0u : 0x3F803F80u;
          if (lane < 2) st128((u32x4{cw, cw, cw, cw}), rs, lane * 320 + 144 + (j - 6) * 16, kvb + 4608 + (4 * (g & 1) + mw) * 640, SC1);
        }
      }
    }
    SBAR();
    f32x4 acc[5];
#pragma unroll
    for (int t = 0; t < 5; ++t) acc[t] = f32x4{0, 0, 0, 0};
    {
      const LAS char* wp = L + WQ_LDS + (288 * kh + 8 * g4 + 2 * ((lane & 15) >> 2)) * 144;
      const LAS char* wa = wp + (lane & 3) * 8 + kh * 64;
      const LAS char* wb = wp + (lane & 3) * 8 + 64 - kh * 64;
      const LAS char* wc = wp + 128 + (lane & 1) * 8;
      u32x4 wfr[9][5];
#pragma unroll
      for (int s = 0; s < 9; ++s) {
        wfr[s][0] = tr_frag<144>(wa + s * 4608); wfr[s][1] = tr_frag<144>(wa + s * 4608 + 32); wfr[s][2] = tr_frag<144>(wc + s * 4608);
        wfr[s][3] = tr_frag<144>(wb + s * 4608); wfr[s][4] = tr_frag<144>(wb + s * 4608 + 32);
        if (s >= 2) {
          SBAR();
#pragma unroll
          for (int t = 0; t < 5; ++t) acc[t] = mfma16(wfr[s - 2][t], xf[s - 2], acc[t]);
        }
        SBAR();
      }
#pragma unroll
      for (int s = 7; s < 9; ++s)
#pragma unroll
        for (int t = 0; t < 5; ++t) acc[t] = mfma16(wfr[s][t], xf[s], acc[t]);
    }
    LAS char* rp = L + KW_LDS + mw * 5120 + l16;
    *(LAS f32x4*)(rp + (2 - 2 * kh) * 1024) = acc[3];
    *(LAS f32x4*)(rp + (3 - 2 * kh) * 1024) = acc[4];
    if (kh) *(LAS f32x4*)(rp + 4 * 1024) = acc[2];
    u32x4 kv[5];
#pragma unroll
    for (int i = 0; i < 5; ++i) kv[i] = __builtin_amdgcn_raw_buffer_load_b128(rs, l16, (int)KV_OFF + (w + 8 * i) * 1024, SC1);
    BAR_LGKM();
    acc[0] += *(const LAS f32x4*)(rp + (2 * kh) * 1024);
    acc[1] += *(const LAS f32x4*)(rp + (2 * kh + 1) * 1024);
    acc[2] += *(const LAS f32x4*)(rp + 4 * 1024);
    const float qs = 0.23570226039551584f * 1.4426950408889634f;
#pragma unroll
    for (int a = 0; a < 3; ++a) {
      if (a < 2 || kh == 0) {
#pragma unroll
        for (int pr = 0; pr < 2; ++pr) *(LAS unsigned*)(L + qoff[2 * a + pr]) = pack2(acc[a][2 * pr] * qs, acc[a][2 * pr + 1] * qs);
      }
    }
    SBAR();
    until_full(kv, 5, rs, l16, (int)KV_OFF + w * 1024, 8192);
#pragma unroll
    for (int i = 0; i < 5; ++i) *(LAS u32x4*)(L + (w + 8 * i) * 1024 + l16) = kv[i];
    epoch1 = __builtin_amdgcn_readfirstlane(epoch) + 1u;
    BAR_LGKM();
  }
  u32x4 wof[12]; u32x2 xv[3]; f32x4 ga[3];
  {
    IDS();
    const int head = 4 * j + mw;
    const u32x4 q1A = *(const LAS u32x4*)(L + QS_LDS + mw * 2048 + l16), q1B = *(const LAS u32x4*)(L + QS_LDS + mw * 2048 + 1024 + l16);
    unsigned qxA = *(const LAS unsigned*)(L + QS_LDS + 8192 + mw * 256 + r32 * 4), qxB = *(const LAS unsigned*)(L + QS_LDS + 8192 + mw * 256 + 128 + r32 * 4);
    u32x4 kv[14];
#define KVL(i) kv[(i) % 14] = __builtin_amdgcn_raw_buffer_load_b128(rs, l16, (int)KV_OFF + (w + 8 * (i)) * 1024, SC1)
#define KVS(i) *(LAS u32x4*)(L + (w + 8 * (i)) * 1024 + l16) = kv[(i) % 14]
    u32x4 csv; unsigned kvm = 0, cwv = 0;
    u32x2 rv0[8], rv1[8];
    const int u0 = g + 32 * w;
    Retile rt0, rt1;
    auto wo_row = [](int k) {
      const int h = k >= 288, kk = k - 288 * h, m = 256 * h + kk;
      return kk < 256 ? (m >> 4) * 18 + (m & 3) + 8 * ((m >> 2) & 1) + 4 * ((m >> 3) & 1) : (16 * h + ((kk - 256) >> 1)) * 18 + 16 + (kk & 1);
    };
    const rsrc_t wor = __builtin_amdgcn_make_buffer_rsrc((void*)wo, 0, DM * DM * 2, 0x00020000);
    const rsrc_t xr = __builtin_amdgcn_make_buffer_rsrc((void*)(x + (size_t)(64 * g + 16 * mw) * DM), 0, 16 * DM * 2, 0x00020000);
    int wva, wvm, xvo;
    auto wo_setup = [&]() {
      int ln = lane; PIN(ln);
      const int l10 = ln < 60 ? ln : 59, lr = l10 / 10, lc = l10 - 10 * lr, cc = (j < 4 || lc < 8) ? lc : 0;
      wva = wo_row(6 * w + lr) * (DM * 2) + cc * 16; wvm = wo_row(6 * w + lr + 240) * (DM * 2) + cc * 16;
      xvo = (ln & 15) * (DM * 2) + (ln >> 4) * 8;
    };
    __builtin_amdgcn_s_setprio(3);
    const int vp = r32 < 17 ? vpos(r32) : r32 == 20 ? 8 : (r32 == 17 || r32 == 21) ? 9 : 10;
    const LAS char* kp = L + kh * KVG + lane * 16;
    const LAS char* kxp = L + kh * KVG + (hi ? 4608 + 9 * 16 : 4096 + r32 * 16);
    const LAS char* vpp = L + kh * KVG + 4608 + hi * 320 + vp * 16;
    int idx = 0xDDDDDDDD;
    PIN(idx); PIN(kp); PIN(kxp); PIN(vpp);
#define LDK(G, t) (*(const LAS u32x4*)(kp + (G) + (t) * 1024))
#define LDX(G) (*(const LAS u32x4*)(kxp + (G)))
#define LDV(G, s) (*(const LAS u32x4*)(vpp + (G) + (s) * 640))
    u32x2 q2A[2], q2B[2];
    float mA, mB;
    f32x16 OA = {}, OB = {};
    {
      make_q2(q2A, qxA, 0.f, hi); make_q2(q2B, qxB, 0.f, hi);
      u32x4 kx = LDX(0), kf = LDK(0, 0), va, vb = {0, 0, 0, 0}, kxn;
      f32x16 A0 = mfma_x<0>(kx, q2A), B0 = mfma_x<0>(kx, q2B), A1 = {}, B1 = {};
      A0 = mfma32(kf, q1A, A0); B0 = mfma32(kf, q1B, B0);
      BAR_LGKM();
      mA = xmax32(rowmax16(A0, -INFINITY)); mB = xmax32(rowmax16(B0, -INFINITY));
      A0 -= mA; B0 -= mB;
      make_q2(q2A, qxA, mA, hi); make_q2(q2B, qxB, mB, hi);
      kf = LDK(0, 1);
      va = LDV(0, 0);
      PIN(A1); PIN(B1); PIN(vb); PIN(OA); PIN(OB);
#define EX4(S, i) do { S[i] = __builtin_amdgcn_exp2f(S[i]); S[i + 1] = __builtin_amdgcn_exp2f(S[i + 1]); \
                       S[i + 2] = __builtin_amdgcn_exp2f(S[i + 2]); S[i + 3] = __builtin_amdgcn_exp2f(S[i + 3]); PIN(S); SBAR(); } while (0)
#define NT_(a) (16 * (t0 + ((a) < 2 ? 2 * kh + (a) : j < 4 ? 4 : 0)))
#define HK(N) do { constexpr int n = (N); \
        if (n < 28 && n % 2 == 0) KVL(5 + n / 2); \
        if (n >= 24 && n < 31) { KVS(5 + n - 24); kvm = umax4(kvm, kv[(5 + n - 24) % 14]); } \
        if (n >= 41 && n < 55 && n % 2 == 1) { KVS(12 + (n - 41) / 2); kvm = umax4(kvm, kv[(12 + (n - 41) / 2) % 14]); } \
        if (n == 66) csv = __builtin_amdgcn_raw_buffer_load_b128(rs, (tid < 432 ? tid : 431) * 16, (int)MOD_OFF + 3 * DM * 4, SC1);     \
        if (n == 126) *(LAS u32x4*)(L + CS_LDS + (tid < 432 ? tid : 431) * 16) = csv; \
        if (n == 114 || n == 118 || n == 122)   \
          st128((u32x4{EMPTY, EMPTY, EMPTY, EMPTY}), rs, w + 2 * (n - 114) < 19 ? l16 : 0x7fffffff, gb0 + (j * 19 + w + 2 * (n - 114)) * 1024, SC1); \
        if (n == 240) cwv = __hip_atomic_load(ctrl + 64 * (hi ? C_F + g : C_W + j), RLX_AGENT); \
        if (n == 32) rt0.init(w1, w2, j, u0, true, lane); \
        if (n == 62) rt1.init(w1, w2, j, u0 + 256 < 324 ? u0 + 256 : u0, u0 + 256 < 324, lane); \
        if (n == 156) wo_setup(); \
        if (n >= 32 && n < 64 && n % 4 == 0) rv0[((n - 32) / 4) & 7] = rt0.load(((n - 32) / 4) & 7); \
        if (n >= 64 && n < 96 && n % 4 == 0) rv1[((n - 64) / 4) & 7] = rt1.load(((n - 64) / 4) & 7); \
        if (n >= 96 && n < 112 && n % 4 == 0) rt0.store(rs, ((n - 96) / 4) & 3, rv0); \
        if (n >= 128 && n < 144 && n % 4 == 0) rt1.store(rs, ((n - 128) / 4) & 3, rv1); \
        if (n >= 160 && n < 220 && n % 5 == 0) { constexpr int i = ((n - 160) / 5 + 36) % 12; \
          wof[i] = __builtin_amdgcn_raw_buffer_load_b128(wor, i % 6 == 5 ? wvm : wva, ((i / 6) * 288 + (i % 6 == 5 ? 0 : (i % 6) * 54)) * (DM * 2) + 32 * t0, 0); } \
        if (n >= 220 && n < 232 && n % 4 == 0) { constexpr int a = ((n - 220) / 4 + 57) % 3; \
          xv[a] = __builtin_amdgcn_raw_buffer_load_b64(xr, xvo, NT_(a) * 2, 0); } \
        if (n >= 222 && n < 234 && n % 4 == 2) { constexpr int a = ((n - 222) / 4 + 57) % 3; \
          ga[a] = __builtin_bit_cast(f32x4, __builtin_amdgcn_raw_buffer_load_b128(rs, g4 * 16, (int)MOD_OFF + (2 * DM + NT_(a)) * 4, SC1)); } \
        if (n == 159) {   \
          asm volatile("s_waitcnt vmcnt(0)" ::: "memory"); \
          if (r32 == 0) __hip_atomic_fetch_add(ctrl + 64 * (hi ? C_F + g : C_W + j), 1u, RLX_AGENT); } \
      } while (0)
#define STEP(AC, BC, AN, BN, KXN, QI, RDVB, RDK, RDVA, EXTRA, N) do { \
      OA = smf_hi(vb, AN, OA, idx); SBAR(); EXTRA; HK(N); EX4(AC, 0); \
      OB = smf_hi(vb, BN, OB, idx); SBAR(); vb = RDVB; HK(N + 1); EX4(AC, 4); \
      AN = mfma_x<QI>(KXN, q2A); SBAR(); HK(N + 2); EX4(BC, 0); \
      BN = mfma_x<QI>(KXN, q2B); SBAR(); HK(N + 3); EX4(BC, 4); \
      OA = smf_lo(va, AC, OA, idx); SBAR(); HK(N + 4); EX4(AC, 8); \
      AN = mfma32(kf, q1A, AN); SBAR(); HK(N + 5); EX4(BC, 8); \
      BN = mfma32(kf, q1B, BN); SBAR(); kf = RDK; HK(N + 6); EX4(AC, 12); \
      OB = smf_lo(va, BC, OB, idx); SBAR(); va = RDVA; HK(N + 7); EX4(BC, 12); } while (0)
#define ITER(IT) do { constexpr int G = (IT) * 2 * KVG, Gn = (IT) == 7 ? 0 : G + 2 * KVG;     \
      STEP(A0, B0, A1, B1, kx, 1, LDV(G, 1), LDK(G, 2), LDV(G, 2), (void)0, 32 * (IT)); \
      STEP(A1, B1, A0, B0, kx, 2, LDV(G, 3), LDK(G, 3), LDV(G, 4), (void)0, 32 * (IT) + 8); \
      STEP(A0, B0, A1, B1, kx, 3, LDV(G, 5), LDK(Gn, 0), LDV(G, 6), kxn = LDX(Gn), 32 * (IT) + 16); \
      STEP(A1, B1, A0, B0, kxn, 0, LDV(G, 7), LDK(Gn, 1), LDV(Gn, 0), (void)0, 32 * (IT) + 24); \
      kx = kxn; } while (0)
      ITER(0);
      auto kv_check = [&](int i0) {
        if (__builtin_expect(!FULL(kvm), 0)) {
#pragma unroll 1
          for (int i = i0; i < i0 + 7; ++i) {
            u32x4 v = {EMPTY, EMPTY, EMPTY, EMPTY};
#pragma unroll 1
            for (unsigned spins = 0; spins < SPIN_CAP && !FULL(umax4(0u, v)); ++spins)
              asm volatile("buffer_load_dwordx4 %0, %1, %2, %3 offen sc1\n\ts_waitcnt vmcnt(0)" : "+v"(v) : "v"(l16), "s"(rs), "s"((int)KV_OFF + (w + 8 * i) * 1024) : "memory");
            *(LAS u32x4*)(L + (w + 8 * i) * 1024 + l16) = v;
          }
        }
        kvm = 0;
        BAR_LGKM();
      };
      kv_check(5);
      ITER(1);
      kv_check(12);
      if (blockIdx.x == 0 && tid == 0) __hip_atomic_store((gu32*)(ws + EPOCH_OFF), epoch1, RLX_AGENT);
      ITER(2); ITER(3);
      __builtin_amdgcn_s_setprio(2);
      ITER(4); ITER(5);
      __builtin_amdgcn_s_setprio(1);
      ITER(6);
      __builtin_amdgcn_s_setprio(0);
      ITER(7);
      OA = smf_hi(vb, A1, OA, idx); OB = smf_hi(vb, B1, OB, idx);
    }
    {
      bool bad = !(OA[9] < 1e18f) || !(OB[9] < 1e18f);
#pragma unroll
      for (int r = 0; r < 9; ++r) bad |= !(fabsf(OA[r]) < 1e37f) || !(fabsf(OB[r]) < 1e37f);
      if (__builtin_expect(__any(bad), 0)) {
        auto redo = [&](const u32x4& q1, unsigned qx, float& m, f32x16& O) {
          PIN(qx);
          u32x2 q2[2];
          auto scores = [&](int t) {
            const int G = (t >> 2) * 2 * KVG;
            const unsigned d = *(const LAS unsigned*)(kxp + G + (t & 3) * 4);
            return mfma32(*(const LAS u32x4*)(kp + G + (t & 3) * 1024), q1, mfma_x<0>(u32x4{d, d, d, d}, q2));
          };
          make_q2(q2, qx, 0.f, hi);
          m = -INFINITY;
#pragma unroll 1
          for (int t = 0; t < 32; ++t) m = rowmax16(scores(t), m);
          m = xmax32(m);
          O = f32x16{};
#pragma unroll 1
          for (int t = 0; t < 32; ++t) {
            f32x16 p = scores(t);
#pragma unroll
            for (int r = 0; r < 16; ++r) p[r] = __builtin_amdgcn_exp2f(p[r] - m);
            const LAS char* vq = vpp + (t >> 2) * 2 * KVG + (t & 3) * 1280;
            O = smf_hi(*(const LAS u32x4*)(vq + 640), p, smf_lo(*(const LAS u32x4*)vq, p, O, idx), idx);
          }
        };
        redo(q1A, qxA, mA, OA);
        redo(q1B, qxB, mB, OB);
      }
    }
    if (w == 0 && !__all(cwv >= (hi ? 64u : 256u))) { poll_ge(ctrl + 64 * (C_W + j), 256u); poll_ge(ctrl + 64 * (C_F + g), 64u); }
    BAR_LGKM();
    {
    IDS();
    const f32x16 Og = kh ? OA : OB, Ok = kh ? OB : OA;
    LAS char* ex = L + w * 3072 + l16;
    *(LAS f32x4*)ex = f32x4{Og[0], Og[1], Og[2], Og[3]};
    *(LAS f32x4*)(ex + 1024) = f32x4{Og[4], Og[5], Og[6], Og[7]};
    *(LAS f32x4*)(ex + 2048) = f32x4{Og[8], Og[9], kh ? mA : mB, 0.f};
    BAR_LGKM();
    const LAS char* ey = L + (w ^ 4) * 3072 + l16;
    const f32x4 p0 = *(const LAS f32x4*)ey, p1 = *(const LAS f32x4*)(ey + 1024), p2 = *(const LAS f32x4*)(ey + 2048);
    const float mo = kh ? mB : mA, mm = fmaxf(mo, p2[2]);
    const float fo = __builtin_amdgcn_exp2f(mo - mm), fp = __builtin_amdgcn_exp2f(p2[2] - mm);
    float O[10];
#pragma unroll
    for (int r = 0; r < 4; ++r) { O[r] = Ok[r] * fo + p0[r] * fp; O[4 + r] = Ok[4 + r] * fo + p1[r] * fp; }
    O[8] = Ok[8] * fo + p2[0] * fp; O[9] = Ok[9] * fo + p2[1] * fp;
    const float rl = __builtin_amdgcn_rcpf(O[9]);
    const int hh = 2 * head + hi;
    u32x4 o = {pack2(O[0] * rl, O[1] * rl), pack2(O[2] * rl, O[3] * rl), pack2(O[4] * rl, O[5] * rl), pack2(O[6] * rl, O[7] * rl)};
    st128(san(o), rs, (r32 >> 4) * 16 * 1024 + (hh & 3) * 256 + (r32 & 15) * 16, gb0 + O_G + (2 * kh * 16 + (head >> 1)) * 1024, SC1);
    const auto es = __builtin_amdgcn_permlane32_swap(__float_as_uint(O[8] * rl), __float_as_uint(O[8] * rl), false, false);
    unsigned ep = san(pack2(__uint_as_float(es[0]), __uint_as_float(es[1])));
    PIN(ep);
    if (!hi) __builtin_amdgcn_raw_buffer_store_b32(ep, rs, r32 * 4, gb0 + OX_G + head * 256 + kh * 128, SC1);
    }
#undef LDK
#undef LDX
#undef LDV
#undef EX4
#undef STEP
#undef ITER
#undef KVL
#undef KVS
#undef HK
#undef NT_
  }
  u32x4 win[WIN];
  {
  IDS();
  {
    const int l10 = lane < 60 ? lane : 59, lr = l10 / 10, lc = l10 - 10 * lr, r0 = 6 * w + lr;
#pragma unroll
    for (int i = 0; i < 12; ++i) *(LAS u32x4*)(L + WI_LDS + (r0 >> 3) * 128 + r0 * 160 + lc * 16 + i * 6 * 1408) = wof[i];
  }
  BAR_LGKM();
    u32x4 of[9];
    const int ovx = g4 * 1024 + (lane & 15) * 4, osx = gb0 + OX_G + 16 * kh * 256 + mw * 64, osm = gb0 + O_G + (mw * 16 + 8 * kh) * 1024;
    u32x4 wfr[9][5];
    load_wfr(wfr, L, lane, kh);
    SBAR();
#pragma unroll
    for (int e = 0; e < 4; ++e) of[8][e] = __builtin_amdgcn_raw_buffer_load_b32(rs, ovx, osx + e * 256, SC1);
#pragma unroll
    for (int s = 0; s < 8; ++s) of[s] = __builtin_amdgcn_raw_buffer_load_b128(rs, l16, osm + s * 1024, SC1);
    SBAR();
    {
      unsigned m = 0;
#pragma unroll
      for (int s = 0; s < 9; ++s) m = umax4(m, of[s]);
      if (!FULL(m)) {
#pragma unroll 1
        for (unsigned spins = 0; spins < SPIN_CAP && !FULL(umax4(0u, of[8])); ++spins)
          asm volatile("buffer_load_dword %0, %4, %5, %6 offen sc1\n\tbuffer_load_dword %1, %4, %5, %6 offen offset:256 sc1\n\t"
                       "buffer_load_dword %2, %4, %5, %6 offen offset:512 sc1\n\tbuffer_load_dword %3, %4, %5, %6 offen offset:768 sc1\n\ts_waitcnt vmcnt(0)"
                       : "=&v"(of[8][0]), "=&v"(of[8][1]), "=&v"(of[8][2]), "=&v"(of[8][3]) : "v"(ovx), "s"(rs), "s"(osx) : "memory");
#pragma unroll 1
        for (unsigned spins = 0; spins < SPIN_CAP; ++spins) {
          asm volatile("buffer_load_dwordx4 %0, %8, %9, %10 offen sc1\n\tbuffer_load_dwordx4 %1, %8, %9, %10 offen offset:1024 sc1\n\t"
                       "buffer_load_dwordx4 %2, %8, %9, %10 offen offset:2048 sc1\n\tbuffer_load_dwordx4 %3, %8, %9, %10 offen offset:3072 sc1\n\t"
                       "buffer_load_dwordx4 %4, %8, %9, %11 offen sc1\n\tbuffer_load_dwordx4 %5, %8, %9, %11 offen offset:1024 sc1\n\t"
                       "buffer_load_dwordx4 %6, %8, %9, %11 offen offset:2048 sc1\n\tbuffer_load_dwordx4 %7, %8, %9, %11 offen offset:3072 sc1\n\ts_waitcnt vmcnt(0)"
                       : "+v"(of[0]), "+v"(of[1]), "+v"(of[2]), "+v"(of[3]), "+v"(of[4]), "+v"(of[5]), "+v"(of[6]), "+v"(of[7])
                       : "v"(l16), "s"(rs), "s"(osm), "s"(osm + 4096) : "memory");
          unsigned m2 = 0;
#pragma unroll
          for (int s = 0; s < 8; ++s) m2 = umax4(m2, of[s]);
          if (FULL(m2)) break;
        }
      }
    }
    f32x4 acc[5];
    small_gemm(acc, wfr, of, L, l16, mw, kh);
    const bool third = kh == 0 && j < 4;
    f32x4 v[3];
    float sum = 0;
#pragma unroll
    for (int a = 0; a < 3; ++a) {
      const int t = a < 2 ? 2 * kh + a : 4, T = t0 + t;
      const f32x4 xx = {bflo(xv[a][0]), bfhi(xv[a][0]), bflo(xv[a][1]), bfhi(xv[a][1])};
      v[a] = __builtin_elementwise_fma(ga[a], acc[a], xx);
      if (a < 2 || third) {
        u32x2 o = {pack2(v[a][0], v[a][1]), pack2(v[a][2], v[a][3])};
        __builtin_amdgcn_raw_buffer_store_b64(san(o), rs, ((g4 >> 1) * 16 + (lane & 15)) * 16 + (g4 & 1) * 8,
                                              gb0 + X1_G + mw * 18 * 1024 + T * 512, SC1);
        sum += (v[a][0] + v[a][1]) + (v[a][2] + v[a][3]);
      }
    }
    sum = xsum_g4(sum);
    const float mu = sum * (third ? 1.f / 48.f : 1.f / 32.f);
    float m2 = 0;
#pragma unroll
    for (int a = 0; a < 3; ++a)
      if (a < 2 || third) {
#pragma unroll
        for (int r = 0; r < 4; ++r) { const float d = v[a][r] - mu; m2 += d * d; }
      }
    m2 = xsum_g4(m2);
    if (g4 == 0) __builtin_amdgcn_raw_buffer_store_b64(san(u32x2{__float_as_uint(sum), __float_as_uint(m2)}), rs, lane * 16,
                                                       gb0 + STAT_G + (j * 64 + 16 * mw) * 16 + kh * 8, SC1);
  }
  {
  IDS();
  const int T0 = w < 4 ? 5 * w : 20 + 4 * (w - 4);
  const rsrc_t wr = __builtin_amdgcn_make_buffer_rsrc(ws + W12_OFF + (size_t)(j * 1296 + (w < 4 ? w * 180 : 720 + (w - 4) * 144)) * 1024, 0, 180 * 1024, 0x00020000);
  f32x4 cs[9][4];
  {
    const LAS float* cp = (const LAS float*)(L + CS_LDS) + 288 * kh + 8 * g4;
#pragma unroll
    for (int s = 0; s < 9; ++s) {
      cs[s][0] = *(const LAS f32x4*)(cp + DM + 32 * s); cs[s][1] = *(const LAS f32x4*)(cp + 32 * s);
      cs[s][2] = *(const LAS f32x4*)(cp + DM + 32 * s + 4); cs[s][3] = *(const LAS f32x4*)(cp + 32 * s + 4);
    }
    SBAR();
  }
  {
    u32x4 xf[9], sva, svb;
    for (unsigned spins = 0; spins < SPIN_CAP; ++spins) {
      sva = __builtin_amdgcn_raw_buffer_load_b128(rs, g4 * 2048 + (lane & 15) * 16, gb0 + STAT_G + 16 * mw * 16, SC1);
      svb = __builtin_amdgcn_raw_buffer_load_b128(rs, g4 * 2048 + (lane & 15) * 16, gb0 + STAT_G + (64 + 16 * mw) * 16, SC1);
      asm volatile("" : "+v"(sva), "+v"(svb)::"memory");
      if (FULL(umax4(umax4(0u, sva), svb))) break;
    }
#pragma unroll
    for (int s = 0; s < 9; ++s) xf[s] = __builtin_amdgcn_raw_buffer_load_b128(rs, l16, gb0 + X1_G + (mw * 18 + 9 * kh + s) * 1024, SC1);
    SBAR();
    {
      const f32x4 sa = __builtin_bit_cast(f32x4, sva), sb = __builtin_bit_cast(f32x4, svb);
      float sum = (sa[0] + sa[2]) + (sb[0] + sb[2]);
      sum = xsum_g4(sum);
      const float mu = sum * (1.0f / DM), cnt = g4 < 2 ? 48.f : 32.f, ic = g4 < 2 ? 1.f / 48.f : 1.f / 32.f;
      const float d0 = sa[0] * ic - mu, d1 = sb[0] * ic - mu, d2 = sa[2] * (1.f / 32.f) - mu, d3 = sb[2] * (1.f / 32.f) - mu;
      const float m2 = xsum_g4((sa[1] + sa[3]) + (sb[1] + sb[3]) + cnt * (d0 * d0 + d1 * d1) + 32.f * (d2 * d2 + d3 * d3));
      const float r = rsqrtf(m2 * (1.0f / DM) + 1e-5f), nmr = -mu * r;
      const f32x4 r4 = {r, r, r, r}, n4 = {nmr, nmr, nmr, nmr};
#pragma unroll
      for (int s = 0; s < 9; ++s) {
        cs[s][1] = __builtin_elementwise_fma(cs[s][0], n4, cs[s][1]); cs[s][0] *= r4;
        cs[s][3] = __builtin_elementwise_fma(cs[s][2], n4, cs[s][3]); cs[s][2] *= r4;
        PIN(cs[s][0]); PIN(cs[s][1]); PIN(cs[s][2]); PIN(cs[s][3]);
      }
      SBAR();
    }
    until_full(xf, 9, rs, l16, gb0 + X1_G + (mw * 18 + 9 * kh) * 1024, 1024);
#pragma unroll
    for (int s = 0; s < 9; ++s) {
      const f32x4 a = {bflo(xf[s][0]), bfhi(xf[s][0]), bflo(xf[s][1]), bfhi(xf[s][1])}, b = {bflo(xf[s][2]), bfhi(xf[s][2]), bflo(xf[s][3]), bfhi(xf[s][3])};
      const f32x4 ha = __builtin_elementwise_fma(a, cs[s][0], cs[s][1]), hb = __builtin_elementwise_fma(b, cs[s][2], cs[s][3]);
      *(LAS u32x4*)(L + ((9 * kh + s) * 4 + mw) * 1024 + l16) = u32x4{pack2(ha[0], ha[1]), pack2(ha[2], ha[3]), pack2(hb[0], hb[1]), pack2(hb[2], hb[3])};
      SBAR();
      if (2 * s < WIN0) win[2 * s] = __builtin_amdgcn_raw_buffer_load_b128(wr, l16, 2 * s * 1024, SC1);
      if (2 * s + 1 < WIN0) win[2 * s + 1] = __builtin_amdgcn_raw_buffer_load_b128(wr, l16, (2 * s + 1) * 1024, SC1);
      SBAR();
    }
    SBAR();
    if ((mw >> 1) == (j >> 2) && ((lane >> 2) & 3) == (j & 3)) {
#pragma unroll
      for (int s = 0; s < 9; ++s) *(LAS u32x4*)(L + XO_LDS + ((mw & 1) * 4 + (lane & 3)) * (DM * 2) + (9 * kh + s) * 64 + g4 * 16) = xf[s];
    }
  }
  BAR_LGKM();
  const int pbase = (int)PART_OFF + (g * 64 + j) * 9216;
  if (w < 4) ffn_phase<5>(L, wr, rs, win, T0, lane, pbase, j);
  else ffn_phase<4>(L, wr, rs, win, T0, lane, pbase, j);
  }
  IDS();
  {
    const int i1 = tid < 64 ? 512 + tid : tid;
    DRAIN_BAR();
    if (tid == 0) { __hip_atomic_fetch_add(ctrl + 64 * (C_P + g), 1u, RLX_AGENT); poll_ge(ctrl + 64 * (C_P + g), 8u); }
    BAR_LGKM();
    u32x4 pv[2][8];
#pragma unroll
    for (int s = 0; s < 7; ++s) pv[0][s] = __builtin_amdgcn_raw_buffer_load_b128(rs, tid * 16, (int)PART_OFF + ((g * 8 + j) * 8 + s + (s >= j)) * 9216, SC1);
    pv[0][7] = *(const LAS u32x4*)(L + OWN_LDS + tid * 16);
    pv[1][7] = *(const LAS u32x4*)(L + OWN_LDS + i1 * 16);
    if (w == 0) {
#pragma unroll
      for (int s = 0; s < 7; ++s) pv[1][s] = __builtin_amdgcn_raw_buffer_load_b128(rs, i1 * 16, (int)PART_OFF + ((g * 8 + j) * 8 + s + (s >= j)) * 9216, SC1);
    }
    SBAR();
    u32x4 xx[2]; f32x4 gq[2][2]; int oo[2], yo[2];
#pragma unroll
    for (int k = 0; k < 2; ++k) {
      const int i = k ? i1 : tid, tk = i / 72, ch = i - 72 * tk;
      xx[k] = *(const LAS u32x4*)(L + XO_LDS + i * 16);
      gq[k][0] = *(const LAS f32x4*)(L + CS_LDS + 2 * DM * 4 + 32 * ch); gq[k][1] = *(const LAS f32x4*)(L + CS_LDS + 2 * DM * 4 + 32 * ch + 16);
      oo[k] = ((64 * g + 32 * (j >> 2) + 16 * (tk >> 2) + 4 * (j & 3) + (tk & 3)) * DM + 8 * ch) * 2;
      yo[k] = tk * 580 + 8 * ch;
      PIN(oo[k]); PIN(yo[k]);
    }
    SBAR();
    LAS float* y = (LAS float*)L;
#pragma unroll
    for (int k = 0; k < 2; ++k) {
      if (k == 0 || w == 0) {
        const int i = k ? i1 : tid, T = i >> 4, c0 = 64 * (T >> 2) + 16 * ((i >> 2) & 3) + (T & 3);
#pragma unroll
        for (int e = 0; e < 4; ++e) {
          float a0 = 0, a1 = 0;
#pragma unroll
          for (int s = 0; s < 8; ++s) { a0 += bflo(pv[k][s][e]); a1 += bfhi(pv[k][s][e]); }
          LAS float* yp = y + ((e >> 1) * 4 + (i & 3)) * 580 + c0 + 8 * (e & 1);
          yp[0] = a0; yp[4] = a1;
        }
      }
    }
    BAR_LGKM();
#pragma unroll
    for (int k = 0; k < 2; ++k) {
      if (k == 0 || w == 0) {
        const f32x4 y0 = *(const LAS f32x4*)(y + yo[k]), y1 = *(const LAS f32x4*)(y + yo[k] + 4);
        const f32x4 a = __builtin_elementwise_fma(gq[k][0], y0, f32x4{bflo(xx[k][0]), bfhi(xx[k][0]), bflo(xx[k][1]), bfhi(xx[k][1])});
        const f32x4 b = __builtin_elementwise_fma(gq[k][1], y1, f32x4{bflo(xx[k][2]), bfhi(xx[k][2]), bflo(xx[k][3]), bfhi(xx[k][3])});
        st128((u32x4{pack2(a[0], a[1]), pack2(a[2], a[3]), pack2(b[0], b[1]), pack2(b[2], b[3])}), __builtin_amdgcn_make_buffer_rsrc(out, 0, SEQ * DM * 2, 0x00020000), oo[k], 0, SC1);
      }
    }
  }
}

extern "C" void kernel_launch(void* const* d_in, const int* in_sizes, int n_in, void* d_out, int out_size, void* d_ws, size_t ws_size,
                              hipStream_t stream) {
  const u16* x = (const u16*)d_in[0];
  const u16* wq = (const u16*)d_in[1];
  const u16* wk = (const u16*)d_in[2];
  const u16* wv = (const u16*)d_in[3];
  const u16* wo = (const u16*)d_in[4];
  const u16* w1 = (const u16*)d_in[6];
  const u16* w2 = (const u16*)d_in[7];
  const u16* c = (const u16*)d_in[8];
  const u16* wmod = (const u16*)d_in[9];
  char* ws = (char*)d_ws;
  k_main<<<256, 512, 0, stream>>>(x, wq, wk, wv, wo, w1, w2, c, wmod, ws, (u16*)d_out);
}
```
